```python
import torch

def _softmax(x, axis=-1):
    return torch.softmax(x, dim=axis)

def _gelu(x):
    return torch.nn.functional.gelu(x, approximate='tanh')

def _silu(x):
    return torch.nn.functional.silu(x)

def _sigmoid(x):
    return torch.sigmoid(x)

def _layernorm(x, axis=-1, eps=1e-05):
    m = x.mean(dim=axis, keepdim=True)
    v = x.var(dim=axis, keepdim=True, unbiased=False)
    return (x - m) / torch.sqrt(v + eps)

def _scatter_add(tgt, idx, src, axis=0):
    return tgt.clone().index_add_(axis, idx, src)

def _segment_sum(src, seg, n):
    out = torch.zeros((n,) + src.shape[1:], dtype=src.dtype)
    return out.index_add_(0, seg, src)

def _pad(x, pw):
    flat = tuple((v for pair in reversed(pw) for v in pair))
    return torch.nn.functional.pad(x, flat)
kernel_name = 'adaLN_cond__compressed_kv_cla__glu_gated_geglu__decode'

def setup_inputs():

    _w = lambda a: (lambda t: t.to(torch.bfloat16) if t.is_floating_point() else t)(torch.as_tensor(a))

    def _A_setup():
        B, H, Sq, Sk, D, M = (2, 32, 1, 8192, 128, 512)
        Dc = 4 * D
        return {'x': _w((lambda *s: torch.randn(*s, dtype=torch.bfloat16) * 0.05)(B, Sq, M)), 'c_kv': _w((lambda *s: torch.randn(*s, dtype=torch.bfloat16) * 3.2)(B, Sk, Dc)), 'w_q': _w((lambda *s: torch.randn(*s, dtype=torch.bfloat16) * 0.8)(M, H, D) * M ** (-0.5)), 'w_uk': _w((lambda *s: torch.randn(*s, dtype=torch.bfloat16) * 0.8)(Dc, H, D) * Dc ** (-0.5)), 'w_uv': _w((lambda *s: torch.randn(*s, dtype=torch.bfloat16) * 3.2)(Dc, H, D) * Dc ** (-0.5)), 'w_o': _w((lambda *s: torch.randn(*s, dtype=torch.bfloat16) * 3.2)(H, D, M) * (H * D) ** (-0.5))}
    _A_in = _A_setup()

    def _B_setup():
        B, S, Dm, Df = (2, 1, 512, 1792)
        return {'x': _w((lambda *s: torch.randn(*s, dtype=torch.bfloat16) * 0.05)(B, S, Dm)), 'w_gate': _w((lambda *s: torch.randn(*s, dtype=torch.bfloat16) * 3.2)(Dm, Df) * Dm ** (-0.5)), 'w_up': _w((lambda *s: torch.randn(*s, dtype=torch.bfloat16) * 3.2)(Dm, Df) * Dm ** (-0.5)), 'w_down': _w((lambda *s: torch.randn(*s, dtype=torch.bfloat16) * 3.2)(Df, Dm) * Df ** (-0.5))}
    _B_in = _B_setup()
    return {'A_x': _A_in['x'], 'A_c_kv': _A_in['c_kv'], 'A_w_q': _A_in['w_q'], 'A_w_uk': _A_in['w_uk'], 'A_w_uv': _A_in['w_uv'], 'A_w_o': _A_in['w_o'], 'B_x': _B_in['x'], 'B_w_gate': _B_in['w_gate'], 'B_w_up': _B_in['w_up'], 'B_w_down': _B_in['w_down'], 'c': _w((lambda *s: torch.randn(*s, dtype=torch.bfloat16) * 0.1)(2, 512) * 8.0), 'w_mod': _w((lambda *s: torch.randn(*s, dtype=torch.bfloat16) * 0.1)(512, 6 * 512) * 512 ** (-0.5) * 8.0)}

def reference(A_x, A_c_kv, A_w_q, A_w_uk, A_w_uv, A_w_o, B_x, B_w_gate, B_w_up, B_w_down, c, w_mod):

    def _A_ref(x, c_kv, w_q, w_uk, w_uv, w_o):
        B, Sq, M = (x.shape[0], x.shape[1], x.shape[2])
        Sk = c_kv.shape[1]
        H, D = (w_q.shape[1], w_q.shape[2])
        q = torch.einsum('bqm,mhd->bhqd', x, w_q)
        k = torch.einsum('bkc,chd->bhkd', c_kv, w_uk)
        v = torch.einsum('bkc,chd->bhkd', c_kv, w_uv)
        scores = torch.einsum('bhqd,bhkd->bhqk', q, k) * D ** (-0.5)
        off = Sk - Sq
        qi = torch.arange(Sq)[:, None]
        kj = torch.arange(Sk)[None, :]
        cmask = kj <= qi + off
        scores = torch.where(cmask[None, None], scores, -1000000000.0)
        p = _softmax(scores, axis=-1)
        ctx = torch.einsum('bhqk,bhkd->bhqd', p, v)
        return torch.einsum('bhqd,hdm->bqm', ctx, w_o)

    def _B_ref(x, w_gate, w_up, w_down):
        g = torch.einsum('bsd,df->bsf', x, w_gate)
        u = torch.einsum('bsd,df->bsf', x, w_up)
        h = _gelu(g) * u
        return torch.einsum('bsf,fd->bsd', h, w_down)
    x = A_x
    mod = (_silu(c) @ w_mod).reshape(c.shape[0], 6, 1, -1)
    sa, ca, ga, sb, cb, gb = (mod[:, 0].clone(), mod[:, 1].clone(), mod[:, 2].clone(), mod[:, 3].clone(), mod[:, 4].clone(), mod[:, 5].clone())
    h = _layernorm(x, axis=-1) * (1 + ca) + sa
    x = x + ga * _A_ref(h, A_c_kv, A_w_q, A_w_uk, A_w_uv, A_w_o)
    h = _layernorm(x, axis=-1) * (1 + cb) + sb
    x = x + gb * _B_ref(h, B_w_gate, B_w_up, B_w_down)
    return x
```

```cpp
#include <hip/hip_runtime.h>
#include <hip/hip_bf16.h>
#include <stdint.h>

typedef unsigned short u16;
__device__ __forceinline__ float bf2f(u16 b) { return __builtin_bit_cast(float, (unsigned)b << 16); }
__device__ __forceinline__ u16 f2bf(float f) { __hip_bfloat16 h = __float2bfloat16(f); return __builtin_bit_cast(u16, h); }

constexpr int B = 2, H = 32, SK = 8192, D = 128, M = 512, DC = 512, DF = 1792;

__global__ void k_mod(const u16* c, const u16* wmod, float* mod) {
  int n = blockIdx.x * 64 + (threadIdx.x & 63);
  int part = threadIdx.x >> 6;
  __shared__ float red[4][2][64];
  float a0 = 0, a1 = 0;
  for (int k = part * 128; k < part * 128 + 128; ++k) {
    float c0 = bf2f(c[k]), c1 = bf2f(c[512 + k]);
    c0 = c0 / (1.f + __expf(-c0)); c1 = c1 / (1.f + __expf(-c1));
    float w = bf2f(wmod[(size_t)k * 3072 + n]);
    a0 += c0 * w; a1 += c1 * w;
  }
  red[part][0][threadIdx.x & 63] = a0; red[part][1][threadIdx.x & 63] = a1;
  __syncthreads();
  if (threadIdx.x < 128) {
    int b = threadIdx.x >> 6, l = threadIdx.x & 63;
    mod[b * 3072 + blockIdx.x * 64 + l] = red[0][b][l] + red[1][b][l] + red[2][b][l] + red[3][b][l];
  }
}

__device__ void ln_mod(const float* xin  , const float* mod, int so, int co, float* hout  ) {
  int w = threadIdx.x >> 6, l = threadIdx.x & 63;
  if (w < 2) {
    float s = 0;
    for (int i = l; i < 512; i += 64) s += xin[w * 512 + i];
    for (int o = 32; o; o >>= 1) s += __shfl_xor(s, o);
    float mean = s / 512.f;
    float v = 0;
    for (int i = l; i < 512; i += 64) { float d = xin[w * 512 + i] - mean; v += d * d; }
    for (int o = 32; o; o >>= 1) v += __shfl_xor(v, o);
    float r = 1.f / sqrtf(v / 512.f + 1e-5f);
    for (int i = l; i < 512; i += 64)
      hout[w * 512 + i] = (xin[w * 512 + i] - mean) * r * (1.f + mod[w * 3072 + co + i]) + mod[w * 3072 + so + i];
  }
  __syncthreads();
}

__global__ void k_q(const u16* x, const float* mod, const u16* wq, float* q) {
  __shared__ float xs[1024], hs[1024];
  __shared__ float red[4][2][64];
  for (int i = threadIdx.x; i < 1024; i += 256) xs[i] = bf2f(x[i]);
  __syncthreads();
  ln_mod(xs, mod, 0, 512, hs);
  int n = blockIdx.x * 64 + (threadIdx.x & 63), part = threadIdx.x >> 6;
  float a0 = 0, a1 = 0;
  for (int k = part * 128; k < part * 128 + 128; ++k) {
    float w = bf2f(wq[(size_t)k * 4096 + n]);
    a0 += hs[k] * w; a1 += hs[512 + k] * w;
  }
  red[part][0][threadIdx.x & 63] = a0; red[part][1][threadIdx.x & 63] = a1;
  __syncthreads();
  if (threadIdx.x < 128) {
    int b = threadIdx.x >> 6, l = threadIdx.x & 63;
    q[b * 4096 + blockIdx.x * 64 + l] = red[0][b][l] + red[1][b][l] + red[2][b][l] + red[3][b][l];
  }
}

__global__ void k_qabs(const float* q, const u16* wuk, float* qa) {
  int wid = blockIdx.x * 4 + (threadIdx.x >> 6), l = threadIdx.x & 63;
  int c = wid / 32, h = wid % 32;
  const u16* w = wuk + (size_t)c * 4096 + h * 128;
  float w0 = bf2f(w[2 * l]), w1 = bf2f(w[2 * l + 1]);
  for (int b = 0; b < 2; ++b) {
    float s = w0 * q[b * 4096 + h * 128 + 2 * l] + w1 * q[b * 4096 + h * 128 + 2 * l + 1];
    for (int o = 32; o; o >>= 1) s += __shfl_xor(s, o);
    if (l == 0) qa[(b * 32 + h) * 512 + c] = s * 0.08838834764831845f;
  }
}

__global__ void k_scores(const float* qa, const u16* ckv, float* sc) {
  int wid = blockIdx.x * 4 + (threadIdx.x >> 6), l = threadIdx.x & 63;
  int b = wid / SK, k = wid % SK;
  float cv[8];
  for (int i = 0; i < 8; ++i) cv[i] = bf2f(ckv[(size_t)wid * 512 + l * 8 + i]);
  for (int h = 0; h < 32; ++h) {
    float s = 0;
    for (int i = 0; i < 8; ++i) s += cv[i] * qa[(b * 32 + h) * 512 + l * 8 + i];
    for (int o = 32; o; o >>= 1) s += __shfl_xor(s, o);
    if (l == 0) sc[(size_t)(b * 32 + h) * SK + k] = s;
  }
}

__global__ void k_softmax(float* sc) {
  float* s = sc + (size_t)blockIdx.x * SK;
  __shared__ float r[4];
  float m = -1e30f;
  for (int i = threadIdx.x; i < SK; i += 256) m = fmaxf(m, s[i]);
  for (int o = 32; o; o >>= 1) m = fmaxf(m, __shfl_xor(m, o));
  if ((threadIdx.x & 63) == 0) r[threadIdx.x >> 6] = m;
  __syncthreads();
  m = fmaxf(fmaxf(r[0], r[1]), fmaxf(r[2], r[3]));
  __syncthreads();
  float t = 0;
  for (int i = threadIdx.x; i < SK; i += 256) { float e = __expf(s[i] - m); s[i] = e; t += e; }
  for (int o = 32; o; o >>= 1) t += __shfl_xor(t, o);
  if ((threadIdx.x & 63) == 0) r[threadIdx.x >> 6] = t;
  __syncthreads();
  t = 1.f / (r[0] + r[1] + r[2] + r[3]);
  for (int i = threadIdx.x; i < SK; i += 256) s[i] *= t;
}

__global__ void k_pv(const float* p, const u16* ckv, float* part) {
  int bh = blockIdx.x, sp = blockIdx.y, b = bh / 32;
  float a0 = 0, a1 = 0;
  for (int k = sp * 256; k < sp * 256 + 256; ++k) {
    float pp = p[(size_t)bh * SK + k];
    unsigned v = *(const unsigned*)(ckv + ((size_t)b * SK + k) * 512 + threadIdx.x * 2);
    a0 += pp * bf2f(v & 0xffff); a1 += pp * bf2f(v >> 16);
  }
  part[((size_t)sp * 64 + bh) * 512 + threadIdx.x * 2] = a0;
  part[((size_t)sp * 64 + bh) * 512 + threadIdx.x * 2 + 1] = a1;
}

__global__ void k_uv(const float* part, const u16* wuv, float* ctx) {
  int bh = blockIdx.x, h = bh % 32;
  __shared__ float cc[512];
  for (int c = threadIdx.x; c < 512; c += 128) {
    float s = 0;
    for (int sp = 0; sp < 32; ++sp) s += part[((size_t)sp * 64 + bh) * 512 + c];
    cc[c] = s;
  }
  __syncthreads();
  float a = 0;
  for (int c = 0; c < 512; ++c) a += cc[c] * bf2f(wuv[(size_t)c * 4096 + h * 128 + threadIdx.x]);
  ctx[bh * 128 + threadIdx.x] = a;
}

__global__ void k_o(const float* ctx, const u16* wo, const u16* x, const float* mod, float* x1) {
  int l = threadIdx.x & 63, part = threadIdx.x >> 6;
  int n = blockIdx.x * 64 + l;
  __shared__ float red[16][2][64];
  float a0 = 0, a1 = 0;
  for (int k = part * 256; k < part * 256 + 256; ++k) {
    float w = bf2f(wo[(size_t)k * 512 + n]);
    a0 += ctx[k] * w; a1 += ctx[4096 + k] * w;
  }
  red[part][0][l] = a0; red[part][1][l] = a1;
  __syncthreads();
  if (threadIdx.x < 128) {
    int b = threadIdx.x >> 6;
    float s = 0;
    for (int i = 0; i < 16; ++i) s += red[i][b][l];
    x1[b * 512 + n] = bf2f(x[b * 512 + n]) + mod[b * 3072 + 1024 + n] * s;
  }
}

__global__ void k_gu(const float* x1, const float* mod, const u16* wg, const u16* wu, float* hh) {
  __shared__ float xs[1024], hs[1024];
  __shared__ float red[4][4][64];
  for (int i = threadIdx.x; i < 1024; i += 256) xs[i] = x1[i];
  __syncthreads();
  ln_mod(xs, mod, 3 * 512, 4 * 512, hs);
  int l = threadIdx.x & 63, n = blockIdx.x * 64 + l, part = threadIdx.x >> 6;
  float g0 = 0, g1 = 0, u0 = 0, u1 = 0;
  for (int k = part * 128; k < part * 128 + 128; ++k) {
    float a = bf2f(wg[(size_t)k * DF + n]), b = bf2f(wu[(size_t)k * DF + n]);
    g0 += hs[k] * a; g1 += hs[512 + k] * a; u0 += hs[k] * b; u1 += hs[512 + k] * b;
  }
  red[part][0][l] = g0; red[part][1][l] = g1; red[part][2][l] = u0; red[part][3][l] = u1;
  __syncthreads();
  if (threadIdx.x < 128) {
    int b = threadIdx.x >> 6;
    float g = red[0][b][l] + red[1][b][l] + red[2][b][l] + red[3][b][l];
    float u = red[0][2 + b][l] + red[1][2 + b][l] + red[2][2 + b][l] + red[3][2 + b][l];
    float t = tanhf(0.7978845608028654f * (g + 0.044715f * g * g * g));
    hh[b * DF + n] = 0.5f * g * (1.f + t) * u;
  }
}

__global__ void k_down(const float* hh, const u16* wd, const float* x1, const float* mod, u16* out) {
  int l = threadIdx.x & 63, part = threadIdx.x >> 6;
  int n = blockIdx.x * 64 + l;
  __shared__ float red[16][2][64];
  float a0 = 0, a1 = 0;
  for (int k = part * 112; k < part * 112 + 112; ++k) {
    float w = bf2f(wd[(size_t)k * 512 + n]);
    a0 += hh[k] * w; a1 += hh[DF + k] * w;
  }
  red[part][0][l] = a0; red[part][1][l] = a1;
  __syncthreads();
  if (threadIdx.x < 128) {
    int b = threadIdx.x >> 6;
    float s = 0;
    for (int i = 0; i < 16; ++i) s += red[i][b][l];
    out[b * 512 + n] = f2bf(x1[b * 512 + n] + mod[b * 3072 + 5 * 512 + n] * s);
  }
}

extern "C" void kernel_launch(void* const* d_in, const int* in_sizes, int n_in,
                              void* d_out, int out_size, void* d_ws, size_t ws_size,
                              hipStream_t stream) {
  const u16* x = (const u16*)d_in[0];
  const u16* ckv = (const u16*)d_in[1];
  const u16* wq = (const u16*)d_in[2];
  const u16* wuk = (const u16*)d_in[3];
  const u16* wuv = (const u16*)d_in[4];
  const u16* wo = (const u16*)d_in[5];
  const u16* wg = (const u16*)d_in[7];
  const u16* wu = (const u16*)d_in[8];
  const u16* wd = (const u16*)d_in[9];
  const u16* c = (const u16*)d_in[10];
  const u16* wmod = (const u16*)d_in[11];
  float* ws = (float*)d_ws;
  float* mod = ws;
  float* q = ws + 8192;
  float* qa = ws + 16384;
  float* ctx = ws + 49152;
  float* x1 = ws + 57344;
  float* hh = ws + 58368;
  float* sc = ws + 65536;
  float* part = sc + 64 * 8192;
  k_mod<<<48, 256, 0, stream>>>(c, wmod, mod);
  k_q<<<64, 256, 0, stream>>>(x, mod, wq, q);
  k_qabs<<<512 * 32 / 4, 256, 0, stream>>>(q, wuk, qa);
  k_scores<<<2 * SK / 4, 256, 0, stream>>>(qa, ckv, sc);
  k_softmax<<<64, 256, 0, stream>>>(sc);
  k_pv<<<dim3(64, 32), 256, 0, stream>>>(sc, ckv, part);
  k_uv<<<64, 128, 0, stream>>>(part, wuv, ctx);
  k_o<<<8, 1024, 0, stream>>>(ctx, wo, x, mod, x1);
  k_gu<<<28, 256, 0, stream>>>(x1, mod, wg, wu, hh);
  k_down<<<8, 1024, 0, stream>>>(hh, wd, x1, mod, (u16*)d_out);
}
```

```cpp
#include <hip/hip_runtime.h>
#include <hip/hip_bf16.h>
#include <stdint.h>

typedef unsigned short u16;
typedef unsigned int u32;
typedef __attribute__((ext_vector_type(4))) u32 u32x4;
typedef __attribute__((ext_vector_type(2))) u32 u32x2;
typedef __attribute__((ext_vector_type(4))) float f32x4;
typedef __attribute__((ext_vector_type(2))) float f32x2;
typedef __attribute__((ext_vector_type(8))) __bf16 bf16x8;
typedef __attribute__((ext_vector_type(2))) __bf16 bf16x2;
typedef __attribute__((ext_vector_type(4))) short s16x4;
#define LAS __attribute__((address_space(3)))
#define GAS __attribute__((address_space(1)))
#define DI __device__ __forceinline__

constexpr u32 OFF_EPOCH = 0;
constexpr u32 OFF_FLAGS = 4096;
constexpr u32 OFF_QP = 32768;
constexpr u32 OFF_QA = OFF_QP + 524288;
constexpr u32 OFF_LATE = OFF_QA + 524288;
constexpr u32 OFF_OP = OFF_LATE;
constexpr u32 OFF_ML = OFF_OP + 4194304;
constexpr u32 OFF_OUTP = OFF_ML + 262144;
constexpr u32 OFF_FIN = OFF_OUTP + 1048576;
constexpr u32 OFF_X1 = OFF_FIN + 262144;
constexpr u32 OFF_MODP = OFF_X1 + 32768;
constexpr u32 LATE_KB_PER_B = 46;
constexpr u32 WS_BYTES = OFF_LATE + 128 * LATE_KB_PER_B * 1024;
static_assert(OFF_MODP + 8192 <= WS_BYTES, "late region");
constexpr u32 OFF_DBG = 8u << 20;
constexpr u32 SENT = 0xFFFFFFFFu;
constexpr int SPIN_MAX = 1 << 16;

constexpr u32 L_C = 0;
constexpr u32 L_Q = 131072;
constexpr u32 L_RED = L_Q;
constexpr u32 L_HB = L_Q + 4096;
constexpr u32 L_H2 = L_Q + 5120;
constexpr u32 L_R4 = L_Q + 7168;
constexpr u32 L_OC = L_Q + 9216;
constexpr u32 L_CTX = L_Q + 11264;
constexpr u32 L_MR = L_Q + 12288;
constexpr u32 L_P = L_Q + 16384;
constexpr u32 L_MX = L_P + 4352;
constexpr u32 L_LS = L_MX + 512;
constexpr u32 L_QB = L_LS + 512;
constexpr u32 LDS_BYTES = L_QB + 512;

DI float bflo(u32 v) { return __builtin_bit_cast(float, v << 16); }
DI float bfhi(u32 v) { return __builtin_bit_cast(float, v & 0xFFFF0000u); }
DI u32 pk2(float a, float b) {
  f32x2 f = {a, b};
  return __builtin_bit_cast(u32, __builtin_convertvector(f, bf16x2));
}
DI float rb(float a) { return bflo(pk2(a, a)); }
DI u32 guard(u32 v) { return v == SENT ? 0x7FC07FC0u : v; }
DI u32 guardf(float v) { u32 u = __builtin_bit_cast(u32, v); return u == SENT ? 0x7FC00000u : u; }
DI f32x4 mfma(u32x4 a, u32x4 b, f32x4 c) {
  return __builtin_amdgcn_mfma_f32_16x16x32_bf16(__builtin_bit_cast(bf16x8, a), __builtin_bit_cast(bf16x8, b), c, 0, 0, 0);
}
template <int CTRL> DI float dpp(float x) {
  return __builtin_bit_cast(float, __builtin_amdgcn_update_dpp(0, __builtin_bit_cast(int, x), CTRL, 0xF, 0xF, true));
}
DI float x16_sum(float x) { auto s = __builtin_amdgcn_permlane16_swap(__float_as_uint(x), __float_as_uint(x), false, false); return __uint_as_float(s[0]) + __uint_as_float(s[1]); }
DI float x32_sum(float x) { auto s = __builtin_amdgcn_permlane32_swap(__float_as_uint(x), __float_as_uint(x), false, false); return __uint_as_float(s[0]) + __uint_as_float(s[1]); }
DI float x16_max(float x) { auto s = __builtin_amdgcn_permlane16_swap(__float_as_uint(x), __float_as_uint(x), false, false); return fmaxf(__uint_as_float(s[0]), __uint_as_float(s[1])); }
DI float x32_max(float x) { auto s = __builtin_amdgcn_permlane32_swap(__float_as_uint(x), __float_as_uint(x), false, false); return fmaxf(__uint_as_float(s[0]), __uint_as_float(s[1])); }
DI float wave_sum(float x) {
  x += dpp<0xB1>(x); x += dpp<0x4E>(x); x += dpp<0x141>(x); x += dpp<0x140>(x);
  return x32_sum(x16_sum(x));
}
DI float wave_max(float x) {
  x = fmaxf(x, dpp<0xB1>(x)); x = fmaxf(x, dpp<0x4E>(x)); x = fmaxf(x, dpp<0x141>(x)); x = fmaxf(x, dpp<0x140>(x));
  return x32_max(x16_max(x));
}
DI float silu_f(float x) { return x * __builtin_amdgcn_rcpf(1.f + __expf(-x)); }
DI float gelu_f(float g) {
  float z = 0.7978845608028654f * (g + 0.044715f * g * g * g);
  float e = __expf(2.f * z);
  float t = 1.f - 2.f * __builtin_amdgcn_rcpf(e + 1.f);
  return 0.5f * g * (1.f + t);
}
DI u32x4 gld16(const void* p) { return *(const GAS u32x4*)p; }
DI u32x2 gld8(const void* p) { return *(const GAS u32x2*)p; }
DI u32 gld2(const void* p) { return *(const GAS u16*)p; }
typedef __amdgpu_buffer_rsrc_t rsrc_t;
DI u32x4 wld16(rsrc_t r, u32 off) { return __builtin_amdgcn_raw_buffer_load_b128(r, off, 0, 16); }
DI u32x2 wld8(rsrc_t r, u32 off) { return __builtin_amdgcn_raw_buffer_load_b64(r, off, 0, 16); }
DI u32 wld4(rsrc_t r, u32 off) { return __builtin_amdgcn_raw_buffer_load_b32(r, off, 0, 16); }
DI void wst16(rsrc_t r, u32 off, u32x4 v) { __builtin_amdgcn_raw_buffer_store_b128(v, r, off, 0, 16); }
DI void wst8(rsrc_t r, u32 off, u32x2 v) { __builtin_amdgcn_raw_buffer_store_b64(v, r, off, 0, 16); }
DI void wst4(rsrc_t r, u32 off, u32 v) { __builtin_amdgcn_raw_buffer_store_b32(v, r, off, 0, 16); }
DI u32 umax(u32 a, u32 b) { return a > b ? a : b; }
DI bool nd4(u32x4 v) { return umax(umax(v[0], v[1]), umax(v[2], v[3])) == SENT; }
DI bool nd2(u32x2 v) { return umax(v[0], v[1]) == SENT; }
DI u32 opq(u32 v) { asm volatile("" : "+v"(v)); return v; }
template <class T> DI void pin(T& v) { asm volatile("" : "+v"(v)); }
DI float rcp(float x) { return __builtin_amdgcn_rcpf(x); }
DI void fence_c() { asm volatile("" ::: "memory"); }
DI void sched_fence() { __builtin_amdgcn_sched_barrier(0); }
DI void lds_wait() { asm volatile("s_waitcnt lgkmcnt(0)" ::: "memory"); }
DI void bar() { asm volatile("s_waitcnt lgkmcnt(0)\n\ts_barrier" ::: "memory"); }
DI void tr8x8(const u32x4 (&W)[8], u32x4 (&F)[8]) {
#pragma unroll
  for (int i = 0; i < 8; ++i)
#pragma unroll
    for (int p = 0; p < 4; ++p)
      F[i][p] = __builtin_amdgcn_perm(W[2 * p + 1][i >> 1], W[2 * p][i >> 1], (i & 1) ? 0x07060302u : 0x05040100u);
}
DI void tr8x4(const u32x2* W, u32x4* F) {
#pragma unroll
  for (int i = 0; i < 4; ++i)
#pragma unroll
    for (int p = 0; p < 4; ++p)
      F[i][p] = __builtin_amdgcn_perm(W[2 * p + 1][i >> 1], W[2 * p][i >> 1], (i & 1) ? 0x07060302u : 0x05040100u);
}
DI void ln_stats(u32x4 v, float& mean, float& sd) {
  float s = 0.f;
#pragma unroll
  for (int i = 0; i < 4; ++i) s += bflo(v[i]) + bfhi(v[i]);
  float m = wave_sum(s) * (1.f / 512.f);
  float q = 0.f;
#pragma unroll
  for (int i = 0; i < 4; ++i) { float a = bflo(v[i]) - m, b = bfhi(v[i]) - m; q += a * a + b * b; }
  float var = wave_sum(q) * (1.f / 512.f);
  mean = rb(m);
  sd = __builtin_amdgcn_rcpf(rb(__builtin_amdgcn_sqrtf(rb(rb(var) + 1e-5f))));
}
DI float ln_mod(float x, float mean, float sd, float sc, float sh) {
  float ln = rb(rb(x - mean) * sd);
  return rb(rb(ln * rb(1.f + sc)) + sh);
}

#define TS(i) do {} while (0)

__global__ void __launch_bounds__(512, 2)
fused_kernel(const u16* __restrict__ x, const u16* __restrict__ ckv, const u16* __restrict__ wq, const u16* __restrict__ wuk,
             const u16* __restrict__ wuv, const u16* __restrict__ wo, const u16* __restrict__ wg, const u16* __restrict__ wu,
             const u16* __restrict__ wd, const u16* __restrict__ cnd, const u16* __restrict__ wmod, u16* __restrict__ out,
             char* ws) {
  extern __shared__ __attribute__((aligned(16))) char smem_raw[];
  LAS char* sm = (LAS char*)smem_raw;
  const int tid = threadIdx.x, l = tid & 63;
  const int w = __builtin_amdgcn_readfirstlane(tid >> 6);
  const int n = l & 15, g = l >> 4;
  const int bid = blockIdx.x, xcd = bid & 7, j = bid >> 3, cls = j & 1, pairidx = j >> 1;
  const int chunk = xcd + 8 * pairidx;
  const rsrc_t R = __builtin_amdgcn_make_buffer_rsrc(ws, 0, WS_BYTES, 0x00020000);
  const u32x4 S4 = {SENT, SENT, SENT, SENT};
  const u32x2 S2v = {SENT, SENT};
  TS(0);
  u32 epoch = 0;
  u32x4 cf[16];
  const u16* cbase = ckv + (size_t)(chunk * 128 + 16 * w + n) * 512 + 8 * g;

  auto poll_flags = [&]() {
    for (int it = 0; it < SPIN_MAX; ++it) {
      u32x4 f = wld16(R, opq(OFF_FLAGS + (u32)(bid & 15) * 1024 + l * 16));
      u32 df = (f[0] ^ epoch) | (f[1] ^ epoch) | (f[2] ^ epoch) | (f[3] ^ epoch);
      if (!__any(df != 0u)) break;
      __builtin_amdgcn_s_sleep(1);
    }
    fence_c();
    if (bid == 0 && l == 0) wst4(R, OFF_EPOCH, epoch);
  };

  if (cls == 0) {
    const int mg = 2 * xcd + (pairidx & 1), cg = pairidx >> 1;
    const int h2 = 4 * xcd + (pairidx >> 2), cq = pairidx & 3;
    auto qa_off = [&](int ww) -> u32 {
      return OFF_QA + (u32)(n >> 1) * 65536 + (u32)((((n & 1) * 2 + (h2 >> 4)) * 16 + cq * 4 + (ww >> 1)) * 1024) +
             (u32)(((h2 & 15) + 16 * (2 * (ww & 1) + (g >> 1))) * 16 + (g & 1) * 8);
    };
    if (w == 0) {
#pragma unroll
      for (int i = 0; i < 4; ++i) wst16(R, OFF_QP + (u32)(((cg * 4 + i) * 16 + mg) * 1024 + l * 16), S4);
#pragma unroll
      for (int i = 0; i < 8; ++i) wst8(R, qa_off(i), S2v);
      fence_c(); sched_fence();
      epoch = wld4(R, OFF_EPOCH);
      fence_c(); sched_fence();
    }
    const int colblk = n & 7, kg = n >> 3;
    u32x4 cfr = gld16(cnd + (n & 1) * 512 + 64 * w + 32 * ((n >> 1) & 1) + 8 * g);
    u32x4 W[8];
    {
      const u16* p = wmod + (size_t)(64 * w + 32 * kg + 8 * g) * 3072 + (colblk < 4 ? 0 : 480) + mg * 32 + 8 * colblk;
#pragma unroll
      for (int i = 0; i < 8; ++i) W[i] = gld16(p + i * 3072);
    }
    u32x4 xv0 = gld16(x + 8 * l), xv1 = gld16(x + 512 + 8 * l);
    u32 xme = gld2(x + (l >> 5) * 512 + mg * 32 + (l & 31));
    u32x2 Wq[8];
    {
      const u16* p = wq + (size_t)(mg * 32 + 8 * g) * 4096 + cg * 512 + 64 * w + 4 * n;
#pragma unroll
      for (int i = 0; i < 8; ++i) Wq[i] = gld8(p + i * 4096);
    }
    u32x4 Wk[4];
    {
      const u16* p = wuk + (size_t)(cq * 128 + 16 * w + n) * 4096 + h2 * 128 + 8 * g;
#pragma unroll
      for (int i = 0; i < 4; ++i) Wk[i] = gld16(p + 32 * i);
    }
    sched_fence();
    if (w == 0) {
      pin(epoch);
      epoch += 1u;
      if (l < 16) wst4(R, OFF_FLAGS + l * 1024 + bid * 4, epoch);
    }
    sched_fence();
    u32x4 af;
#pragma unroll
    for (int i = 0; i < 4; ++i) af[i] = pk2(silu_f(bflo(cfr[i])), silu_f(bfhi(cfr[i])));
    u32x4 F[8];
    tr8x8(W, F);
    float v0[8], v1[8];
#pragma unroll
    for (int i = 0; i < 8; ++i) {
      f32x4 a = mfma(af, F[i], (f32x4){0.f, 0.f, 0.f, 0.f});
      float a0 = kg ? a[2] : a[0], a1 = kg ? a[3] : a[1];
      v0[i] = a0 + dpp<0x128>(a0);
      v1[i] = a1 + dpp<0x128>(a1);
    }
    if (l < 8) {
      LAS f32x4* r = (LAS f32x4*)(sm + L_RED + w * 512 + l * 32);
      r[0] = (f32x4){v0[0], v0[1], v0[2], v0[3]}; r[1] = (f32x4){v0[4], v0[5], v0[6], v0[7]};
      r[16] = (f32x4){v1[0], v1[1], v1[2], v1[3]}; r[17] = (f32x4){v1[4], v1[5], v1[6], v1[7]};
    }
    float mean0, sd0, mean1, sd1;
    ln_stats(xv0, mean0, sd0);
    ln_stats(xv1, mean1, sd1);
    u32x4 Fq[4];
    tr8x4(Wq, Fq);
    pin(mean0); pin(sd0); pin(mean1); pin(sd1);
#pragma unroll
    for (int i = 0; i < 4; ++i) pin(Fq[i]);
    TS(1);
    bar();
    {
      const int b = l >> 5, f = l & 31;
      const LAS float* r = (const LAS float*)(sm + L_RED + b * 256 + f * 4);
      float sa = 0.f, ca = 0.f;
#pragma unroll
      for (int i = 0; i < 8; ++i) { sa += r[i * 128]; ca += r[i * 128 + 32]; }
      float hv = ln_mod(bflo(xme), b ? mean1 : mean0, b ? sd1 : sd0, rb(ca), rb(sa));
      *(LAS u16*)(sm + L_HB + w * 128 + l * 2) = (u16)(pk2(hv, hv) & 0xFFFFu);
      lds_wait();
      u32x4 ha = *(const LAS u32x4*)(sm + L_HB + w * 128 + (n & 1) * 64 + g * 16);
      u32x4 o;
#pragma unroll
      for (int i = 0; i < 4; ++i) {
        f32x4 a = mfma(ha, Fq[i], (f32x4){0.f, 0.f, 0.f, 0.f});
        o[i] = guardf((g & 1) ? a[1] : a[0]);
      }
      if (g < 2) wst16(R, OFF_QP + (u32)((((cg * 4 + (w >> 1)) * 16 + mg) * 2 + g) * 512 + (64 * (w & 1) + 4 * n) * 4), o);
    }
    TS(2);
    if (w == 0) {
      poll_flags();
      u32x4 qv[16];
      const u32 qo = OFF_QP + (u32)h2 * 16384 + l * 16;
#pragma unroll
      for (int i = 0; i < 16; ++i) qv[i] = wld16(R, qo + i * 1024);
      for (int it = 0; it < SPIN_MAX; ++it) {
        bool nd[16], any = false;
#pragma unroll
        for (int i = 0; i < 16; ++i) { nd[i] = nd4(qv[i]); any |= nd[i]; }
        if (!__any(any)) break;
        const u32 o = opq(qo);
#pragma unroll
        for (int i = 0; i < 16; ++i) if (nd[i]) qv[i] = wld16(R, o + i * 1024);
      }
      f32x4 s = {0.f, 0.f, 0.f, 0.f};
#pragma unroll
      for (int i = 0; i < 16; ++i) s += __builtin_bit_cast(f32x4, qv[i]);
      *(LAS u32x2*)(sm + L_QB + l * 8) = (u32x2){pk2(s[0], s[1]), pk2(s[2], s[3])};
    }
    bar();
    TS(3);
    {
      f32x4 a = {0.f, 0.f, 0.f, 0.f};
#pragma unroll
      for (int ks = 0; ks < 4; ++ks) {
        u32x4 qf = *(const LAS u32x4*)(sm + L_QB + (n & 1) * 256 + ks * 64 + g * 16);
        a = mfma(Wk[ks], qf, a);
      }
      wst8(R, qa_off(w), (u32x2){guard(pk2(a[0], a[1])), guard(pk2(a[2], a[3]))});
    }
    sched_fence();
#pragma unroll
    for (int ks = 0; ks < 16; ++ks) cf[ks] = gld16(cbase + 32 * ks);
    sched_fence();
    TS(4);
  } else {
    u32 modv = 0;
    if (w == 0) {
      const u32 o = OFF_LATE + (u32)chunk * (LATE_KB_PER_B * 1024) + l * 16;
#pragma unroll
      for (int i = 0; i < (int)LATE_KB_PER_B; ++i) wst16(R, o + i * 1024, S4);
      fence_c(); sched_fence();
      epoch = wld4(R, OFF_EPOCH);
      fence_c(); sched_fence();
#pragma unroll
      for (int ks = 0; ks < 16; ++ks) cf[ks] = gld16(cbase + 32 * ks);
      sched_fence();
      pin(epoch);
      epoch += 1u;
      if (l < 16) wst4(R, OFF_FLAGS + l * 1024 + bid * 4, epoch);
      TS(1);
      poll_flags();
    } else if (w == 1) {
      u32x4 W0[8], W1[8];
      const u16* p = wmod + (size_t)(32 * (n >> 1) + 8 * g) * 3072 + 1024 + 16 * chunk + 8 * (n & 1);
      u32x4 c0 = gld16(cnd + (n & 1) * 512 + 32 * (n >> 1) + 8 * g), c1 = gld16(cnd + (n & 1) * 512 + 256 + 32 * (n >> 1) + 8 * g);
#pragma unroll
      for (int i = 0; i < 8; ++i) W0[i] = gld16(p + i * 3072);
#pragma unroll
      for (int i = 0; i < 8; ++i) W1[i] = gld16(p + (256 + i) * 3072);
#pragma unroll
      for (int ks = 0; ks < 16; ++ks) cf[ks] = gld16(cbase + 32 * ks);
      sched_fence();
      u32x4 a0, a1, F[8];
#pragma unroll
      for (int i = 0; i < 4; ++i) { a0[i] = pk2(silu_f(bflo(c0[i])), silu_f(bfhi(c0[i]))); a1[i] = pk2(silu_f(bflo(c1[i])), silu_f(bfhi(c1[i]))); }
      f32x4 acc[8];
      tr8x8(W0, F);
#pragma unroll
      for (int i = 0; i < 8; ++i) acc[i] = mfma(a0, F[i], (f32x4){0.f, 0.f, 0.f, 0.f});
      tr8x8(W1, F);
#pragma unroll
      for (int i = 0; i < 8; ++i) acc[i] = mfma(a1, F[i], acc[i]);
      if (g == (n >> 2)) {
        const bool odd = (n >> 1) & 1;
        LAS f32x4* r = (LAS f32x4*)(sm + L_MR + (n >> 1) * 128 + (n & 1) * 32);
        f32x4 t0, t1, t2, t3;
#pragma unroll
        for (int i = 0; i < 4; ++i) {
          t0[i] = odd ? acc[i][2] : acc[i][0]; t1[i] = odd ? acc[4 + i][2] : acc[4 + i][0];
          t2[i] = odd ? acc[i][3] : acc[i][1]; t3[i] = odd ? acc[4 + i][3] : acc[4 + i][1];
        }
        r[0] = t0; r[1] = t1; r[4] = t2; r[5] = t3;
      }
      lds_wait();
      {
        const LAS f32x2* r = (const LAS f32x2*)(sm + L_MR + (l & 15) * 8);
        f32x2 s = {0.f, 0.f};
#pragma unroll
        for (int i = 0; i < 8; ++i) s += r[i * 16];
        modv = guard(pk2(s[0], s[1]));
      }
    } else {
#pragma unroll
      for (int ks = 0; ks < 16; ++ks) cf[ks] = gld16(cbase + 32 * ks);
      sched_fence();
    }
    TS(2);
    bar();
    if (w == 1 && l < 16) wst4(R, OFF_MODP + (u32)((l >> 3) * 4096 + chunk * 32 + (l & 7) * 4), modv);
  }

  const int h4 = 4 * xcd + (j >> 3), cs = j & 7;
  {
    const int row = 16 * w + n;
    const u32 swz = ((row & 3) << 2) | ((row >> 2) & 3);
    if (cls == 1) {
#pragma unroll
      for (int ks = 0; ks < 16; ++ks) *(LAS u32x4*)(sm + L_C + row * 1024 + (((4 * ks + g) ^ swz) * 16)) = cf[ks];
    }
    TS(5);
    const u32 qo = OFF_QA + (u32)xcd * 65536 + (u32)((((chunk >> 6) * 2 + cls) * 16 + 2 * w) * 1024) + l * 16;
    u32x4 q0 = wld16(R, qo), q1 = wld16(R, qo + 1024);
    if (cls == 0) {
#pragma unroll
      for (int ks = 0; ks < 16; ++ks) *(LAS u32x4*)(sm + L_C + row * 1024 + (((4 * ks + g) ^ swz) * 16)) = cf[ks];
    }
    for (int it = 0; it < SPIN_MAX; ++it) {
      const bool n0 = nd4(q0), n1 = nd4(q1);
      if (!__any(n0 | n1)) break;
      __builtin_amdgcn_s_sleep(1);
      const u32 o = opq(qo);
      if (n0) q0 = wld16(R, o);
      if (n1) q1 = wld16(R, o + 1024);
    }
    *(LAS u32x4*)(sm + L_Q + (2 * w) * 1024 + l * 16) = q0;
    *(LAS u32x4*)(sm + L_Q + (2 * w + 1) * 1024 + l * 16) = q1;
  }
  u32x2 Wo[32];
  u32 Wv[16];
  {
    const u16* p = wuv + (size_t)(cs * 64 + 32 * (g & 1) + 4 * (g >> 1)) * 4096 + h4 * 128 + 16 * w + n;
#pragma unroll
    for (int ks = 0; ks < 2; ++ks)
#pragma unroll
      for (int jj = 0; jj < 8; ++jj) Wv[ks * 8 + jj] = gld2(p + (size_t)(8 * ks + 16 * (jj >> 2) + (jj & 3)) * 4096);
    const u16* po = wo + (size_t)(h4 * 128 + 8 * g) * 512 + 64 * w + 4 * n;
#pragma unroll
    for (int i = 0; i < 32; ++i) Wo[i] = gld8(po + (32 * (i >> 3) + (i & 7)) * 512);
  }
  sched_fence();
  bar();
  TS(6);
  {
    f32x4 s0 = {0.f, 0.f, 0.f, 0.f}, s1 = {0.f, 0.f, 0.f, 0.f};
    u32x4 qf[16];
#pragma unroll
    for (int ks = 0; ks < 16; ++ks) qf[ks] = *(const LAS u32x4*)(sm + L_Q + ks * 1024 + l * 16);
    sched_fence();
#pragma unroll
    for (int ks = 0; ks < 16; ks += 2) {
      s0 = mfma(cf[ks], qf[ks], s0);
      s1 = mfma(cf[ks + 1], qf[ks + 1], s1);
    }
    float sc[4];
#pragma unroll
    for (int r = 0; r < 4; ++r) sc[r] = rb(rb(s0[r] + s1[r]) * 0.08838834764831845f);
    float mx = fmaxf(fmaxf(sc[0], sc[1]), fmaxf(sc[2], sc[3]));
    mx = x32_max(x16_max(mx));
    if (g == 0) *(LAS float*)(sm + L_MX + n * 32 + w * 4) = mx;
    bar();
    {
      f32x4 m0 = *(const LAS f32x4*)(sm + L_MX + n * 32), m1 = *(const LAS f32x4*)(sm + L_MX + n * 32 + 16);
      mx = fmaxf(fmaxf(fmaxf(m0[0], m0[1]), fmaxf(m0[2], m0[3])), fmaxf(fmaxf(m1[0], m1[1]), fmaxf(m1[2], m1[3])));
    }
    float p[4];
#pragma unroll
    for (int r = 0; r < 4; ++r) p[r] = __expf(sc[r] - mx);
    *(LAS u32x2*)(sm + L_P + n * 272 + (16 * w + 4 * g) * 2) = (u32x2){pk2(p[0], p[1]), pk2(p[2], p[3])};
    float ls = x32_sum(x16_sum((p[0] + p[1]) + (p[2] + p[3])));
    if (g == 0) *(LAS float*)(sm + L_LS + n * 32 + w * 4) = ls;
    bar();
    TS(7);
    f32x4 o[4];
#pragma unroll
    for (int t = 0; t < 4; ++t) o[t] = (f32x4){0.f, 0.f, 0.f, 0.f};
    const int q = n >> 2, pp = n & 3;
    u32x4 pf[4], ca[16];
#pragma unroll
    for (int ks = 0; ks < 4; ++ks) {
      pf[ks] = *(const LAS u32x4*)(sm + L_P + n * 272 + (32 * ks + 8 * g) * 2);
#pragma unroll
      for (int t = 0; t < 4; ++t) {
#pragma unroll
        for (int tt = 0; tt < 2; ++tt) {
          const int row = 32 * ks + 8 * g + 4 * tt + q;
          const u32 swz = (q << 2) | ((2 * g + tt) & 3);
          const u32 ad = L_C + row * 1024 + (((8 * w + 2 * t + (pp >> 1)) ^ swz) * 16) + 8 * (pp & 1);
          u32x2 v = __builtin_bit_cast(u32x2, __builtin_amdgcn_ds_read_tr16_b64_v4i16((LAS s16x4*)(sm + ad)));
          ca[4 * ks + t][2 * tt] = v[0]; ca[4 * ks + t][2 * tt + 1] = v[1];
        }
      }
    }
    sched_fence();
#pragma unroll
    for (int ks = 0; ks < 4; ++ks)
#pragma unroll
      for (int t = 0; t < 4; ++t) o[t] = mfma(ca[4 * ks + t], pf[ks], o[t]);
    const u32 rec = (u32)(((16 * cls + n) * 8 + w) * 128 + chunk);
    wst16(R, OFF_OP + rec * 128 + g * 32,
          (u32x4){guard(pk2(o[0][0], o[0][1])), guard(pk2(o[0][2], o[0][3])), guard(pk2(o[1][0], o[1][1])), guard(pk2(o[1][2], o[1][3]))});
    wst16(R, OFF_OP + rec * 128 + g * 32 + 16,
          (u32x4){guard(pk2(o[2][0], o[2][1])), guard(pk2(o[2][2], o[2][3])), guard(pk2(o[3][0], o[3][1])), guard(pk2(o[3][2], o[3][3]))});
    {
      f32x4 l0 = *(const LAS f32x4*)(sm + L_LS + n * 32), l1 = *(const LAS f32x4*)(sm + L_LS + n * 32 + 16);
      float lt = ((l0[0] + l0[1]) + (l0[2] + l0[3])) + ((l1[0] + l1[1]) + (l1[2] + l1[3]));
      if (g == 0) wst8(R, OFF_ML + rec * 8, (u32x2){guardf(mx), guardf(lt)});
    }
  }
  TS(8);
  {
    const u32 base = (u32)(h4 * 8 + cs) * 128;
    const u32 s0i = 16 * w + (l >> 3);
    const u32 oo = OFF_OP + (base + s0i) * 128 + (l & 7) * 16;
    const u32 mo = OFF_ML + (base + 64 * (w >> 2) + l) * 8;
    const u32 mo0 = OFF_ML + (base + s0i) * 8;
    u32x4 o0 = wld16(R, oo), o1 = wld16(R, oo + 1024);
    u32x2 ma = wld8(R, mo), m0 = wld8(R, mo0), m1 = wld8(R, mo0 + 64);
    sched_fence();
    u32x4 Fv[2], Fo[16];
#pragma unroll
    for (int ks = 0; ks < 2; ++ks)
#pragma unroll
      for (int i = 0; i < 4; ++i) Fv[ks][i] = Wv[ks * 8 + 2 * i] | (Wv[ks * 8 + 2 * i + 1] << 16);
#pragma unroll
    for (int ks = 0; ks < 4; ++ks) tr8x4(Wo + 8 * ks, Fo + 4 * ks);
#pragma unroll
    for (int i = 0; i < 16; ++i) pin(Fo[i]);
    pin(Fv[0]); pin(Fv[1]);
    for (int it = 0; it < SPIN_MAX; ++it) {
      const bool n0 = nd4(o0), n1 = nd4(o1), n2 = nd2(ma), n3 = nd2(m0), n4 = nd2(m1);
      if (!__any(n0 | n1 | n2 | n3 | n4)) break;
      const u32 z = opq(0u);
      if (n0) o0 = wld16(R, oo + z);
      if (n1) o1 = wld16(R, oo + z + 1024);
      if (n2) ma = wld8(R, mo + z);
      if (n3) m0 = wld8(R, mo0 + z);
      if (n4) m1 = wld8(R, mo0 + z + 64);
    }
    TS(9);
    const float mm = __uint_as_float(ma[0]);
    const float M = wave_max(mm);
    const float Ls = wave_sum(__expf(mm - M) * __uint_as_float(ma[1]));
    const float inv = rcp(Ls);
    const float w0 = __expf(__uint_as_float(m0[0]) - M) * inv, w1 = __expf(__uint_as_float(m1[0]) - M) * inv;
    float a[8];
#pragma unroll
    for (int i = 0; i < 4; ++i) {
      a[2 * i] = w0 * bflo(o0[i]) + w1 * bflo(o1[i]);
      a[2 * i + 1] = w0 * bfhi(o0[i]) + w1 * bfhi(o1[i]);
    }
#pragma unroll
    for (int i = 0; i < 8; ++i) { a[i] += dpp<0x128>(a[i]); a[i] = x32_sum(x16_sum(a[i])); }
    if (l < 8) {
      LAS f32x4* r = (LAS f32x4*)(sm + L_R4 + w * 256 + l * 32);
      r[0] = (f32x4){a[0], a[1], a[2], a[3]}; r[1] = (f32x4){a[4], a[5], a[6], a[7]};
    }
    bar();
    {
      const LAS f32x2* r = (const LAS f32x2*)(sm + L_R4 + (l >> 5) * 1024 + (l & 31) * 8);
      f32x2 s = (r[0] + r[32]) + (r[64] + r[96]);
      *(LAS u32*)(sm + L_OC + w * 256 + l * 4) = pk2(s[0], s[1]);
      lds_wait();
      u32x4 b0 = *(const LAS u32x4*)(sm + L_OC + w * 256 + (n & 1) * 128 + g * 16);
      u32x4 b1 = *(const LAS u32x4*)(sm + L_OC + w * 256 + (n & 1) * 128 + 64 + g * 16);
      f32x4 c = mfma(Fv[0], b0, (f32x4){0.f, 0.f, 0.f, 0.f});
      c = mfma(Fv[1], b1, c);
      if (n < 2) *(LAS u32x2*)(sm + L_CTX + n * 256 + (16 * w + 4 * g) * 2) = (u32x2){pk2(c[0], c[1]), pk2(c[2], c[3])};
    }
    bar();
    {
      f32x4 acc[4];
#pragma unroll
      for (int i = 0; i < 4; ++i) acc[i] = (f32x4){0.f, 0.f, 0.f, 0.f};
#pragma unroll
      for (int ks = 0; ks < 4; ++ks) {
        u32x4 ca = *(const LAS u32x4*)(sm + L_CTX + (n & 1) * 256 + ks * 64 + g * 16);
#pragma unroll
        for (int i = 0; i < 4; ++i) acc[i] = mfma(ca, Fo[4 * ks + i], acc[i]);
      }
      u32x4 o;
#pragma unroll
      for (int i = 0; i < 4; ++i) o[i] = guardf((g & 1) ? acc[i][1] : acc[i][0]);
      if (g < 2) wst16(R, OFF_OUTP + (u32)(g * 128 + 16 * w + n) * 4096 + bid * 16, o);
    }
  }
  TS(10);
  if (cls == 0) {
    if (w < 2) {
      const int u = 2 * chunk + w, b = u >> 7, mb = u & 127;
      const u32 oo = OFF_OUTP + (u32)u * 4096 + l * 16;
      const u32 go = OFF_MODP + b * 4096 + mb * 8;
      u32x4 v[4];
#pragma unroll
      for (int i = 0; i < 4; ++i) v[i] = wld16(R, oo + i * 1024);
      u32x2 ga = wld8(R, go), gb = wld8(R, go + 3072);
      u32x2 xv = gld8(x + b * 512 + mb * 4);
      for (int it = 0; it < SPIN_MAX; ++it) {
        bool nd[4];
#pragma unroll
        for (int i = 0; i < 4; ++i) nd[i] = nd4(v[i]);
        const bool ng = nd2(ga) | nd2(gb);
        if (!__any(nd[0] | nd[1] | nd[2] | nd[3] | ng)) break;
        const u32 z = opq(0u);
#pragma unroll
        for (int i = 0; i < 4; ++i) if (nd[i]) v[i] = wld16(R, oo + z + i * 1024);
        if (ng) { ga = wld8(R, go + z); gb = wld8(R, go + z + 3072); }
      }
      TS(11);
      f32x4 s = (__builtin_bit_cast(f32x4, v[0]) + __builtin_bit_cast(f32x4, v[1])) + (__builtin_bit_cast(f32x4, v[2]) + __builtin_bit_cast(f32x4, v[3]));
      float x1[4];
#pragma unroll
      for (int i = 0; i < 4; ++i) {
        float at = rb(wave_sum(s[i]));
        float gv = (i & 1) ? bfhi(ga[i >> 1]) : bflo(ga[i >> 1]);
        float xx = (i & 1) ? bfhi(xv[i >> 1]) : bflo(xv[i >> 1]);
        x1[i] = rb(xx + rb(gv * at));
      }
      if (l < 16) wst8(R, OFF_X1 + l * 2048 + b * 1024 + mb * 8, (u32x2){guard(pk2(x1[0], x1[1])), guard(pk2(x1[2], x1[3]))});
      TS(12);
      const u32 fo = OFF_FIN + (u32)u * 1024 + l * 16;
      u32x4 f = {0u, 0u, 0u, 0u};
      if (l < 56) f = wld16(R, fo);
      for (int it = 0; it < SPIN_MAX; ++it) {
        const bool nf = nd4(f);
        if (!__any(nf)) break;
        __builtin_amdgcn_s_sleep(1);
        if (nf) f = wld16(R, opq(fo));
      }
      TS(13);
      float r[4];
#pragma unroll
      for (int i = 0; i < 4; ++i) {
        float y = rb(wave_sum(__uint_as_float(f[i])));
        float gv = (i & 1) ? bfhi(gb[i >> 1]) : bflo(gb[i >> 1]);
        r[i] = x1[i] + rb(gv * y);
      }
      if (l == 0) *(GAS u32x2*)(out + b * 512 + mb * 4) = (u32x2){pk2(r[0], r[1]), pk2(r[2], r[3])};
      TS(14);
    }
  } else if (chunk < 56) {
    const int colblk = n & 7, kg = n >> 3;
    u32x4 sbv, cbv;
    const u32 so = OFF_MODP + (u32)(w & 1) * 4096 + 1024 + l * 16;
    if (w < 2) { sbv = wld16(R, so); cbv = wld16(R, so + 1024); }
    u32x4 W[8];
    {
      const u16* p = (colblk < 4 ? wg : wu) + (size_t)(64 * w + 32 * kg + 8 * g) * 1792 + chunk * 32 + 8 * (colblk & 3);
#pragma unroll
      for (int i = 0; i < 8; ++i) W[i] = gld16(p + i * 1792);
    }
    u32x2 Wd[8];
    {
      const u16* p = wd + (size_t)(chunk * 32 + 8 * g) * 512 + 64 * w + 4 * n;
#pragma unroll
      for (int i = 0; i < 8; ++i) Wd[i] = gld8(p + i * 512);
    }
    sched_fence();
    u32x4 F[8], Fd[4];
    tr8x8(W, F);
    tr8x4(Wd, Fd);
#pragma unroll
    for (int i = 0; i < 8; ++i) pin(F[i]);
#pragma unroll
    for (int i = 0; i < 4; ++i) pin(Fd[i]);
    if (w < 2) {
      for (int it = 0; it < SPIN_MAX; ++it) {
        if (!__any(nd4(sbv) | nd4(cbv))) break;
        const u32 o = opq(so);
        sbv = wld16(R, o); cbv = wld16(R, o + 1024);
      }
      float c1[8], sh[8];
#pragma unroll
      for (int i = 0; i < 4; ++i) {
        c1[2 * i] = rb(1.f + bflo(cbv[i])); c1[2 * i + 1] = rb(1.f + bfhi(cbv[i]));
        sh[2 * i] = bflo(sbv[i]); sh[2 * i + 1] = bfhi(sbv[i]);
      }
#pragma unroll
      for (int i = 0; i < 8; ++i) { pin(c1[i]); pin(sh[i]); }
      TS(11);
      const u32 xo = OFF_X1 + (u32)(chunk & 15) * 2048 + (u32)w * 1024 + l * 16;
      u32x4 a0 = wld16(R, xo);
      for (int it = 0; it < SPIN_MAX; ++it) {
        const bool n0 = nd4(a0);
        if (!__any(n0)) break;
        if (n0) a0 = wld16(R, opq(xo));
      }
      TS(12);
      float mean, sd;
      ln_stats(a0, mean, sd);
      u32x4 h0;
#pragma unroll
      for (int i = 0; i < 4; ++i)
        h0[i] = pk2(rb(rb(rb(bflo(a0[i]) - mean) * sd) * c1[2 * i]) + sh[2 * i], rb(rb(rb(bfhi(a0[i]) - mean) * sd) * c1[2 * i + 1]) + sh[2 * i + 1]);
      *(LAS u32x4*)(sm + L_H2 + w * 1024 + l * 16) = h0;
    }
    bar();
    {
      u32x4 af = *(const LAS u32x4*)(sm + L_H2 + (n & 1) * 1024 + (64 * w + 32 * ((n >> 1) & 1) + 8 * g) * 2);
      float v0[8], v1[8];
#pragma unroll
      for (int i = 0; i < 8; ++i) {
        f32x4 a = mfma(af, F[i], (f32x4){0.f, 0.f, 0.f, 0.f});
        float a0 = kg ? a[2] : a[0], a1 = kg ? a[3] : a[1];
        v0[i] = a0 + dpp<0x128>(a0);
        v1[i] = a1 + dpp<0x128>(a1);
      }
      if (l < 8) {
        LAS f32x4* r = (LAS f32x4*)(sm + L_RED + w * 512 + l * 32);
        r[0] = (f32x4){v0[0], v0[1], v0[2], v0[3]}; r[1] = (f32x4){v0[4], v0[5], v0[6], v0[7]};
        r[16] = (f32x4){v1[0], v1[1], v1[2], v1[3]}; r[17] = (f32x4){v1[4], v1[5], v1[6], v1[7]};
      }
    }
    bar();
    {
      const LAS float* r = (const LAS float*)(sm + L_RED + (l >> 5) * 256 + (l & 31) * 4);
      float gs = 0.f, us = 0.f;
#pragma unroll
      for (int i = 0; i < 8; ++i) { gs += r[i * 128]; us += r[i * 128 + 32]; }
      float hv = rb(gelu_f(rb(gs))) * rb(us);
      *(LAS u16*)(sm + L_HB + w * 128 + l * 2) = (u16)(pk2(hv, hv) & 0xFFFFu);
      lds_wait();
      u32x4 ha = *(const LAS u32x4*)(sm + L_HB + w * 128 + (n & 1) * 64 + g * 16);
      u32x4 o;
#pragma unroll
      for (int i = 0; i < 4; ++i) {
        f32x4 a = mfma(ha, Fd[i], (f32x4){0.f, 0.f, 0.f, 0.f});
        o[i] = guardf((g & 1) ? a[1] : a[0]);
      }
      if (g < 2) wst16(R, OFF_FIN + (u32)(g * 128 + 16 * w + n) * 1024 + chunk * 16, o);
    }
    TS(13);
  }
}

extern "C" void kernel_launch(void* const* d_in, const int* in_sizes, int n_in, void* d_out, int out_size, void* d_ws,
                              size_t ws_size, hipStream_t stream) {
  (void)in_sizes; (void)n_in; (void)out_size; (void)ws_size;
  (void)hipFuncSetAttribute((const void*)fused_kernel, hipFuncAttributeMaxDynamicSharedMemorySize, (int)LDS_BYTES);
  hipLaunchKernelGGL(fused_kernel, dim3(256), dim3(512), LDS_BYTES, stream, (const u16*)d_in[0], (const u16*)d_in[1],
                     (const u16*)d_in[2], (const u16*)d_in[3], (const u16*)d_in[4], (const u16*)d_in[5], (const u16*)d_in[7],
                     (const u16*)d_in[8], (const u16*)d_in[9], (const u16*)d_in[10], (const u16*)d_in[11], (u16*)d_out, (char*)d_ws);
}
```

```cpp
#include <hip/hip_runtime.h>
#include <hip/hip_bf16.h>
#include <stdint.h>
#include <type_traits>

typedef unsigned short u16;
typedef unsigned int u32;
typedef __attribute__((ext_vector_type(4))) u32 u32x4;
typedef __attribute__((ext_vector_type(2))) u32 u32x2;
typedef __attribute__((ext_vector_type(4))) float f32x4;
typedef __attribute__((ext_vector_type(2))) float f32x2;
typedef __attribute__((ext_vector_type(8))) __bf16 bf16x8;
typedef __attribute__((ext_vector_type(2))) __bf16 bf16x2;
typedef __attribute__((ext_vector_type(4))) short s16x4;
#define LAS __attribute__((address_space(3)))
#define GAS __attribute__((address_space(1)))
#define DI __device__ __forceinline__

constexpr u32 OFF_HINT = 1024;
constexpr u32 OFF_HM = 4096;
constexpr u32 OFF_MODG = OFF_HM + 65536;
constexpr u32 OFF_X1 = OFF_MODG + 131072;
constexpr u32 OFF_QA = OFF_X1 + 32768;
constexpr u32 OFF_QK = OFF_QA + 131072;
constexpr u32 OFF_OP = OFF_QK + 4096;
constexpr u32 OFF_ML = OFF_OP + 4194304;
constexpr u32 OFF_OUTP = OFF_ML + 524288;
constexpr u32 OFF_FIN = OFF_OUTP + 524288;
constexpr u32 WS_BYTES = OFF_FIN + 262144;
constexpr u32 OFF_DBG = 8u << 20;
static_assert(WS_BYTES <= OFF_DBG, "ws");
constexpr int SPIN_MAX = 1 << 16;

constexpr u32 L_C = 0;
constexpr u32 L_Q = 131072;
constexpr u32 L_P = L_Q + 16384;
constexpr u32 L_MX = L_P + 4352;
constexpr u32 L_LS = L_MX + 512;
constexpr u32 L_RED = L_LS + 512;
constexpr u32 L_HB = L_RED + 8192;
constexpr u32 L_QB = L_HB + 2048;
constexpr u32 LDS_BYTES = L_QB + 512;
static_assert(LDS_BYTES <= 163840, "lds");

DI float bflo(u32 v) { return __builtin_bit_cast(float, v << 16); }
DI float bfhi(u32 v) { return __builtin_bit_cast(float, v & 0xFFFF0000u); }
DI u32 pk2(float a, float b) {
  f32x2 f = {a, b};
  return __builtin_bit_cast(u32, __builtin_convertvector(f, bf16x2));
}
DI float rb(float a) { return bflo(pk2(a, a)); }
DI f32x4 mfma(u32x4 a, u32x4 b, f32x4 c) {
  return __builtin_amdgcn_mfma_f32_16x16x32_bf16(__builtin_bit_cast(bf16x8, a), __builtin_bit_cast(bf16x8, b), c, 0, 0, 0);
}
template <int CTRL> DI float dpp(float x) {
  return __builtin_bit_cast(float, __builtin_amdgcn_update_dpp(0, __builtin_bit_cast(int, x), CTRL, 0xF, 0xF, true));
}
DI float x16_sum(float x) { auto s = __builtin_amdgcn_permlane16_swap(__float_as_uint(x), __float_as_uint(x), false, false); return __uint_as_float(s[0]) + __uint_as_float(s[1]); }
DI float x32_sum(float x) { auto s = __builtin_amdgcn_permlane32_swap(__float_as_uint(x), __float_as_uint(x), false, false); return __uint_as_float(s[0]) + __uint_as_float(s[1]); }
DI float x16_max(float x) { auto s = __builtin_amdgcn_permlane16_swap(__float_as_uint(x), __float_as_uint(x), false, false); return fmaxf(__uint_as_float(s[0]), __uint_as_float(s[1])); }
DI float x32_max(float x) { auto s = __builtin_amdgcn_permlane32_swap(__float_as_uint(x), __float_as_uint(x), false, false); return fmaxf(__uint_as_float(s[0]), __uint_as_float(s[1])); }
DI float wave_sum(float x) {
  x += dpp<0xB1>(x); x += dpp<0x4E>(x); x += dpp<0x141>(x); x += dpp<0x140>(x);
  return x32_sum(x16_sum(x));
}
DI float wave_max(float x) {
  x = fmaxf(x, dpp<0xB1>(x)); x = fmaxf(x, dpp<0x4E>(x)); x = fmaxf(x, dpp<0x141>(x)); x = fmaxf(x, dpp<0x140>(x));
  return x32_max(x16_max(x));
}
DI float silu_f(float x) { return x * __builtin_amdgcn_rcpf(1.f + __expf(-x)); }
DI float gelu_f(float g) {
  float z = 0.7978845608028654f * (g + 0.044715f * g * g * g);
  float e = __expf(2.f * z);
  float t = 1.f - 2.f * __builtin_amdgcn_rcpf(e + 1.f);
  return 0.5f * g * (1.f + t);
}
DI u32x4 gld16(const void* p) { return *(const GAS u32x4*)p; }
DI u32x2 gld8(const void* p) { return *(const GAS u32x2*)p; }
DI u32 gld2(const void* p) { return *(const GAS u16*)p; }
typedef __amdgpu_buffer_rsrc_t rsrc_t;
DI u32x4 wld16(rsrc_t r, u32 off) { return __builtin_amdgcn_raw_buffer_load_b128(r, off, 0, 16); }
DI u32x2 wld8(rsrc_t r, u32 off) { return __builtin_amdgcn_raw_buffer_load_b64(r, off, 0, 16); }
DI u32 wld4(rsrc_t r, u32 off) { return __builtin_amdgcn_raw_buffer_load_b32(r, off, 0, 16); }
DI void wst16(rsrc_t r, u32 off, u32x4 v) { __builtin_amdgcn_raw_buffer_store_b128(v, r, off, 0, 16); }
DI void wst8(rsrc_t r, u32 off, u32x2 v) { __builtin_amdgcn_raw_buffer_store_b64(v, r, off, 0, 16); }
DI void wst4(rsrc_t r, u32 off, u32 v) { __builtin_amdgcn_raw_buffer_store_b32(v, r, off, 0, 16); }
DI u32 opq(u32 v) { asm volatile("" : "+v"(v)); return v; }
template <class T> DI void pin(T& v) { asm volatile("" : "+v"(v)); }
DI float rcp(float x) { return __builtin_amdgcn_rcpf(x); }
DI void fence_c() { asm volatile("" ::: "memory"); }
DI void sched_fence() { __builtin_amdgcn_sched_barrier(0); }
DI void lds_wait() { asm volatile("s_waitcnt lgkmcnt(0)" ::: "memory"); }
DI void bar() { asm volatile("s_waitcnt lgkmcnt(0)\n\ts_barrier" ::: "memory"); }
DI void tr8x8(const u32x4 (&W)[8], u32x4 (&F)[8]) {
#pragma unroll
  for (int i = 0; i < 8; ++i)
#pragma unroll
    for (int p = 0; p < 4; ++p)
      F[i][p] = __builtin_amdgcn_perm(W[2 * p + 1][i >> 1], W[2 * p][i >> 1], (i & 1) ? 0x07060302u : 0x05040100u);
}
DI void tr8x4(const u32x2* W, u32x4* F) {
#pragma unroll
  for (int i = 0; i < 4; ++i)
#pragma unroll
    for (int p = 0; p < 4; ++p)
      F[i][p] = __builtin_amdgcn_perm(W[2 * p + 1][i >> 1], W[2 * p][i >> 1], (i & 1) ? 0x07060302u : 0x05040100u);
}
DI void ln_stats(u32x4 v, float& mean, float& sd) {
  float s = 0.f;
#pragma unroll
  for (int i = 0; i < 4; ++i) s += bflo(v[i]) + bfhi(v[i]);
  float m = wave_sum(s) * (1.f / 512.f);
  float q = 0.f;
#pragma unroll
  for (int i = 0; i < 4; ++i) { float a = bflo(v[i]) - m, b = bfhi(v[i]) - m; q += a * a + b * b; }
  float var = wave_sum(q) * (1.f / 512.f);
  mean = rb(m);
  sd = __builtin_amdgcn_rcpf(rb(__builtin_amdgcn_sqrtf(rb(rb(var) + 1e-5f))));
}
DI float ln_mod(float x, float mean, float sd, float sc, float sh) {
  float ln = rb(rb(x - mean) * sd);
  return rb(rb(ln * rb(1.f + sc)) + sh);
}

#define NT_WQ 0
#define NT_CB 0
#define NT_CA 0
#define NT_W1 1
#define HINT_SLEEP 10
#define B_DELAY 0
#define OP_SLEEP 0
#define A_EARLY 0
template <int NT> DI u32x4 gl16(const void* p) {
  if (NT) return __builtin_nontemporal_load((const GAS u32x4*)p);
  return *(const GAS u32x4*)p;
}
template <int NT> DI u32x2 gl8(const void* p) {
  if (NT) return __builtin_nontemporal_load((const GAS u32x2*)p);
  return *(const GAS u32x2*)p;
}
template <int NT> DI void dma16(const void* gp, u32 lds) {
  u32 keep;
  if (NT) asm volatile("s_mov_b32 %0, m0\n\ts_mov_b32 m0, %2\n\ts_nop 0\n\tglobal_load_lds_dwordx4 %1, off nt\n\ts_mov_b32 m0, %0" : "=&s"(keep) : "v"(gp), "s"(lds) : "memory");
  else asm volatile("s_mov_b32 %0, m0\n\ts_mov_b32 m0, %2\n\ts_nop 0\n\tglobal_load_lds_dwordx4 %1, off\n\ts_mov_b32 m0, %0" : "=&s"(keep) : "v"(gp), "s"(lds) : "memory");
}
template <int CTRL> DI u32 dppu(u32 x) { return (u32)__builtin_amdgcn_update_dpp(0, (int)x, CTRL, 0xF, 0xF, true); }
DI u32 sum8l(u32 x) { x += dppu<0xB1>(x); x += dppu<0x4E>(x); x += dppu<0x141>(x); return x; }
DI u32 sum16l(u32 x) { x = sum8l(x); x += dppu<0x140>(x); return x; }
DI bool bad16x2(u32x2 v, u32 t) { return (((v[0] ^ t) | (v[1] ^ t)) & 0xFFFFu) != 0u; }
DI void vm_wait0() { asm volatile("s_waitcnt vmcnt(0)" ::: "memory"); }
DI u32 sum4(u32x4 v) { return (v[0] + v[1]) + (v[2] + v[3]); }
DI bool bad16(u32x4 v, u32 t) { return (((v[0] ^ t) | (v[1] ^ t) | (v[2] ^ t) | (v[3] ^ t)) & 0xFFFFu) != 0u; }
DI bool bad8(u32x4 v, u32 t) { return (((v[0] ^ t) | (v[1] ^ t) | (v[2] ^ t) | (v[3] ^ t)) & 0xFFu) != 0u; }
DI u32 tg16(float bfval, u32 t16) { return (__float_as_uint(bfval) & 0xFFFF0000u) | t16; }
DI u32 tg8(float v, u32 t8) { return (__float_as_uint(v) & 0xFFFFFF00u) | t8; }
DI float un8(u32 v) { return __uint_as_float(v & 0xFFFFFF00u); }

#define TS(i) do {} while (0)

__global__ void __launch_bounds__(512, 2)
fused_kernel(const u16* __restrict__ x, const u16* __restrict__ ckv, const u16* __restrict__ wq, const u16* __restrict__ wuk,
             const u16* __restrict__ wuv, const u16* __restrict__ wo, const u16* __restrict__ wg, const u16* __restrict__ wu,
             const u16* __restrict__ wd, const u16* __restrict__ cnd, const u16* __restrict__ wmod, u16* __restrict__ out,
             char* ws) {
  extern __shared__ __attribute__((aligned(16))) char smem_raw[];
  LAS char* sm = (LAS char*)smem_raw;
  const u32 lds0 = (u32)(size_t)sm;
  const int tid = threadIdx.x, l = tid & 63;
  const int w = __builtin_amdgcn_readfirstlane(tid >> 6);
  const int n = l & 15, g = l >> 4;
  const int bid = blockIdx.x, xcd = bid & 7, j = bid >> 3;
  const int cls = (j >= 2 && j < 18) ? 0 : 1, pairidx = j < 2 ? j : (j < 18 ? j - 2 : j - 16);
  const int chunk = xcd + 8 * pairidx;
  const int h2 = 4 * xcd + (pairidx >> 2), cq = pairidx & 3;
  const int fi = chunk - 72;
  const rsrc_t R = __builtin_amdgcn_make_buffer_rsrc(ws, 0, WS_BYTES, 0x00020000);
  const f32x4 Z4 = {0.f, 0.f, 0.f, 0.f};
  TS(0);
  const u32 Ev = *(const GAS u32*)ws + 1u;
  const int colblk = n & 7, kg = n >> 3;
  u32x2 Wo[32];
  u32x4 Wv[8];
  auto load_c = [&](int i0, int i1, auto nt) {
#pragma unroll
    for (int i = i0; i < i1; ++i) {
      const u32 swz = ((i & 3) << 2) | (i >> 2);
      dma16<decltype(nt)::value>(ckv + (size_t)(chunk * 128 + 16 * w + i) * 512 + (l ^ swz) * 8, lds0 + L_C + (16 * w + i) * 1024);
    }
  };
  auto load_s4 = [&]() {
    if (w < 4) {
      const u16* p = wuv + (size_t)(cq * 128 + (w >> 1) * 64 + 32 * kg + 4 * g) * 4096 + h2 * 128 + 64 * (w & 1) + 8 * colblk;
#pragma unroll
      for (int i = 0; i < 8; ++i) Wv[i] = gl16<NT_W1>(p + (size_t)(16 * (i >> 2) + (i & 3)) * 4096);
    }
    const u16* po = wo + (size_t)(h2 * 128 + 8 * g) * 512 + 64 * w + 4 * n;
#pragma unroll
    for (int i = 0; i < 32; ++i) Wo[i] = gld8(po + (32 * (i >> 3) + (i & 7)) * 512);
  };
  u32 E;

  if (cls == 0) {
    u32x4 xv0 = gld16(x + 8 * l), xv1 = gld16(x + 512 + 8 * l);
    u32 xm0 = gld2(x + tid), xm1 = gld2(x + 512 + tid);
    u32x4 W[16];
    {
      const u16* p = wq + (size_t)(64 * w + 8 * g) * 4096 + h2 * 128 + 8 * n;
#pragma unroll
      for (int i = 0; i < 16; ++i) W[i] = gl16<NT_WQ>(p + (size_t)(32 * (i >> 3) + (i & 7)) * 4096);
    }
#pragma unroll
    for (int i = 0; i < 4; ++i)
      dma16<NT_W1>(wuk + (size_t)(128 * cq + 16 * w + 4 * i + g) * 4096 + h2 * 128 + (n ^ (4 * i + g)) * 8, lds0 + L_C + w * 16384 + 12288 + i * 1024);
    if (A_EARLY) load_c(0, A_EARLY, std::integral_constant<int, NT_CA>());
    const u32 ho = OFF_HM + (u32)xcd * 8192 + tid * 8;
    u32x2 hs = wld8(R, ho), hc = wld8(R, ho + 4096);
    sched_fence();
    E = __builtin_amdgcn_readfirstlane(Ev);
    float mean0, sd0, mean1, sd1;
    ln_stats(xv0, mean0, sd0);
    ln_stats(xv1, mean1, sd1);
    float ln0 = rb(rb(bflo(xm0) - mean0) * sd0), ln1 = rb(rb(bflo(xm1) - mean1) * sd1);
    u32x4 F[16];
    tr8x8(*(const u32x4(*)[8])(W), *(u32x4(*)[8])(F));
    tr8x8(*(const u32x4(*)[8])(W + 8), *(u32x4(*)[8])(F + 8));
#pragma unroll
    for (int i = 0; i < 16; ++i) pin(F[i]);
    pin(ln0); pin(ln1);
    vm_wait0();
    u32x4 uk[4];
#pragma unroll
    for (int ks = 0; ks < 4; ++ks) uk[ks] = *(const LAS u32x4*)(sm + L_C + w * 16384 + 12288 + n * 256 + (((4 * ks + g) ^ n) * 16));
#pragma unroll
    for (int i = 0; i < 4; ++i) pin(uk[i]);
    TS(1);
    for (int it = 0; it < SPIN_MAX; ++it) {
      if (!__any(bad16x2(hs, E) | bad16x2(hc, E))) break;
      const u32 o = opq(ho);
      hs = wld8(R, o); hc = wld8(R, o + 4096);
    }
    TS(2);
    {
      float h0 = rb(ln0 * rb(1.f + bfhi(hc[0]))) + bfhi(hs[0]);
      float h1 = rb(ln1 * rb(1.f + bfhi(hc[1]))) + bfhi(hs[1]);
      u32 hp = pk2(h0, h1);
      *(LAS u16*)(sm + L_HB + w * 256 + l * 2) = (u16)(hp & 0xFFFFu);
      *(LAS u16*)(sm + L_HB + w * 256 + 128 + l * 2) = (u16)(hp >> 16);
      lds_wait();
      u32x4 a0 = *(const LAS u32x4*)(sm + L_HB + w * 256 + (n & 1) * 128 + g * 16);
      u32x4 a1 = *(const LAS u32x4*)(sm + L_HB + w * 256 + (n & 1) * 128 + 64 + g * 16);
      f32x4 acc[8];
#pragma unroll
      for (int i = 0; i < 8; ++i) acc[i] = mfma(a0, F[i], Z4);
#pragma unroll
      for (int i = 0; i < 8; ++i) acc[i] = mfma(a1, F[8 + i], acc[i]);
      if (g == 0) {
        LAS f32x4* r = (LAS f32x4*)(sm + L_RED + w * 1024 + n * 32);
        r[0] = (f32x4){acc[0][0], acc[1][0], acc[2][0], acc[3][0]}; r[1] = (f32x4){acc[4][0], acc[5][0], acc[6][0], acc[7][0]};
        r[32] = (f32x4){acc[0][1], acc[1][1], acc[2][1], acc[3][1]}; r[33] = (f32x4){acc[4][1], acc[5][1], acc[6][1], acc[7][1]};
      }
    }
    bar();
    if (w < 4) {
      const LAS float* r = (const LAS float*)(sm + L_RED + tid * 4);
      float s = ((r[0] + r[256]) + (r[512] + r[768])) + ((r[1024] + r[1280]) + (r[1536] + r[1792]));
      *(LAS u16*)(sm + L_QB + tid * 2) = (u16)(pk2(s, s) & 0xFFFFu);
    }
    bar();
    {
      u32x4 qb[4];
#pragma unroll
      for (int ks = 0; ks < 4; ++ks) qb[ks] = *(const LAS u32x4*)(sm + L_QB + (n & 1) * 256 + ks * 64 + g * 16);
      f32x4 d0 = mfma(uk[0], qb[0], Z4), d1 = mfma(uk[1], qb[1], Z4);
      d0 = mfma(uk[2], qb[2], d0); d1 = mfma(uk[3], qb[3], d1);
      d0 += d1;
      if (n < 2) *(LAS u32x2*)(sm + L_HB + n * 256 + w * 32 + g * 8) = (u32x2){pk2(d0[0], d0[1]), pk2(d0[2], d0[3])};
    }
    bar();
    if (w == 0) {
      const u32x4 pay = *(const LAS u32x4*)(sm + L_HB + (g & 1) * 256 + n * 16);
      wst16(R, OFF_QA + (u32)(g * 32 + h2) * 1024 + cq * 256 + n * 16, pay);
      const u32 ck = sum16l(sum4(pay)) + E;
      if (n == 0) wst4(R, OFF_QK + (u32)(g * 16 + (h2 >> 1)) * 32 + cq * 8 + (h2 & 1) * 4, ck);
    }
    TS(3);
    load_c(A_EARLY, 16, std::integral_constant<int, NT_CA>());
  } else {
    if (chunk < 48) {
      u32x4 cfr = gld16(cnd + (n & 1) * 512 + 64 * w + 32 * ((n >> 1) & 1) + 8 * g);
      u32x4 W[8];
      const u16* p = wmod + (size_t)(64 * w + 32 * kg + 8 * g) * 3072 + chunk * 64 + 8 * colblk;
#pragma unroll
      for (int i = 0; i < 8; ++i) W[i] = gl16<NT_W1>(p + i * 3072);
      sched_fence();
      u32x4 af;
#pragma unroll
      for (int i = 0; i < 4; ++i) af[i] = pk2(silu_f(bflo(cfr[i])), silu_f(bfhi(cfr[i])));
      pin(af);
      u32x4 F[8];
      tr8x8(W, F);
      float v0[8], v1[8];
#pragma unroll
      for (int i = 0; i < 8; ++i) {
        f32x4 a = mfma(af, F[i], Z4);
        float a0 = kg ? a[2] : a[0], a1 = kg ? a[3] : a[1];
        v0[i] = a0 + dpp<0x128>(a0);
        v1[i] = a1 + dpp<0x128>(a1);
      }
      if (l < 8) {
        LAS f32x4* r = (LAS f32x4*)(sm + L_RED + w * 512 + l * 32);
        r[0] = (f32x4){v0[0], v0[1], v0[2], v0[3]}; r[1] = (f32x4){v0[4], v0[5], v0[6], v0[7]};
        r[16] = (f32x4){v1[0], v1[1], v1[2], v1[3]}; r[17] = (f32x4){v1[4], v1[5], v1[6], v1[7]};
      }
      bar();
      TS(1);
      E = __builtin_amdgcn_readfirstlane(Ev);
      const LAS float* r = (const LAS float*)(sm + L_RED + l * 4);
      float m0 = ((r[0] + r[128]) + (r[256] + r[384])) + ((r[512] + r[640]) + (r[768] + r[896]));
      float m1 = ((r[64] + r[192]) + (r[320] + r[448])) + ((r[576] + r[704]) + (r[832] + r[960]));
      const u32 pm = pk2(m0, m1), t16 = E & 0xFFFFu;
      const u32 d0 = (pm << 16) | t16, d1 = (pm & 0xFFFF0000u) | t16;
      const int kind = chunk >> 3, m = (chunk & 7) * 64 + l;
      if (kind < 2) {
        wst8(R, OFF_HM + (u32)w * 8192 + kind * 4096 + m * 8, (u32x2){d0, d1});
        if (l == 0) wst4(R, OFF_HINT + (u32)w * 128 + chunk * 4, E);
      } else {
        const u32 o = OFF_MODG + (u32)w * 16384 + (kind - 2) * 4096 + m * 4;
        wst4(R, o, d0); wst4(R, o + 2048, d1);
      }
      TS(2);
    }
    if (B_DELAY && chunk >= 48) __builtin_amdgcn_s_sleep(B_DELAY);
    load_c(0, 16, std::integral_constant<int, NT_CB>());
    load_s4();
    E = __builtin_amdgcn_readfirstlane(Ev);
    TS(3);
    if (w == 0) {
      const u32 ho = OFF_HINT + (u32)xcd * 128 + n * 4;
      for (int it = 0; it < SPIN_MAX; ++it) {
        if (!__any(wld4(R, opq(ho)) != E)) break;
        __builtin_amdgcn_s_sleep(2);
      }
      __builtin_amdgcn_s_sleep(HINT_SLEEP);
    }
    bar();
  }
  const u32 t8 = E & 0xFFu, t16 = E & 0xFFFFu;
  u32x4 cf[16];
  {
    const u32 rb_ = (u32)(xcd & 1) * 2 + (chunk >> 6);
    const u32 qo = OFF_QA + (rb_ * 32 + 16 * cls + 2 * w) * 1024 + l * 16, ko = OFF_QK + (rb_ * 16 + 8 * cls + w) * 32 + g * 8;
    u32x4 q0 = wld16(R, qo), q1 = wld16(R, qo + 1024);
    u32x2 qk = wld8(R, ko);
    vm_wait0();
    TS(4);
    const u32 swz = ((n & 3) << 2) | (n >> 2);
#pragma unroll
    for (int ks = 0; ks < 16; ++ks) cf[ks] = *(const LAS u32x4*)(sm + L_C + (16 * w + n) * 1024 + (((4 * ks + g) ^ swz) * 16));
#pragma unroll
    for (int ks = 0; ks < 16; ++ks) pin(cf[ks]);
    for (int it = 0; it < SPIN_MAX; ++it) {
      const bool nb = (sum16l(sum4(q0)) + E != qk[0]) | (sum16l(sum4(q1)) + E != qk[1]);
      if (!__any(nb)) break;
      const u32 z = opq(0u);
      q0 = wld16(R, qo + z); q1 = wld16(R, qo + z + 1024); qk = wld8(R, ko + z);
    }
    const u32 z0 = (((2 * w) & 3) << 2) | ((2 * w) >> 2), z1 = (((2 * w + 1) & 3) << 2) | ((2 * w + 1) >> 2);
    *(LAS u32x4*)(sm + L_Q + (2 * w) * 1024 + (l ^ z0) * 16) = q0;
    *(LAS u32x4*)(sm + L_Q + (2 * w + 1) * 1024 + (l ^ z1) * 16) = q1;
  }
  TS(5);
  sched_fence();
  sched_fence();
  bar();
  TS(6);
  {
    f32x4 s0 = Z4, s1 = Z4, s2 = Z4, s3 = Z4;
    const u32 qswz = ((n & 3) << 2) | (n >> 2);
#pragma unroll
    for (int ks = 0; ks < 16; ks += 4) {
      s0 = mfma(cf[ks], *(const LAS u32x4*)(sm + L_Q + n * 1024 + ((4 * ks + g) ^ qswz) * 16), s0);
      s1 = mfma(cf[ks + 1], *(const LAS u32x4*)(sm + L_Q + n * 1024 + ((4 * (ks + 1) + g) ^ qswz) * 16), s1);
      s2 = mfma(cf[ks + 2], *(const LAS u32x4*)(sm + L_Q + n * 1024 + ((4 * (ks + 2) + g) ^ qswz) * 16), s2);
      s3 = mfma(cf[ks + 3], *(const LAS u32x4*)(sm + L_Q + n * 1024 + ((4 * (ks + 3) + g) ^ qswz) * 16), s3);
    }
    s0 = (s0 + s1) + (s2 + s3);
    float sc[4];
#pragma unroll
    for (int r = 0; r < 4; ++r) sc[r] = rb(rb(s0[r]) * 0.08838834764831845f);
    float mx = fmaxf(fmaxf(sc[0], sc[1]), fmaxf(sc[2], sc[3]));
    mx = x32_max(x16_max(mx));
    if (g == 0) *(LAS float*)(sm + L_MX + n * 32 + w * 4) = mx;
    bar();
    const int q = n >> 2, pp = n & 3;
    u32x4 ca[16];
    {
      f32x4 m0 = *(const LAS f32x4*)(sm + L_MX + n * 32), m1 = *(const LAS f32x4*)(sm + L_MX + n * 32 + 16);
      sched_fence();
#pragma unroll
      for (int ks = 0; ks < 4; ++ks)
#pragma unroll
        for (int t = 0; t < 4; ++t)
#pragma unroll
          for (int tt = 0; tt < 2; ++tt) {
            const int row = 32 * ks + 8 * g + 4 * tt + q;
            const u32 swz = (q << 2) | ((2 * g + tt) & 3);
            const u32 ad = L_C + row * 1024 + (((8 * w + 2 * t + (pp >> 1)) ^ swz) * 16) + 8 * (pp & 1);
            u32x2 v = __builtin_bit_cast(u32x2, __builtin_amdgcn_ds_read_tr16_b64_v4i16((LAS s16x4*)(sm + ad)));
            ca[4 * ks + t][2 * tt] = v[0]; ca[4 * ks + t][2 * tt + 1] = v[1];
          }
      sched_fence();
      mx = fmaxf(fmaxf(fmaxf(m0[0], m0[1]), fmaxf(m0[2], m0[3])), fmaxf(fmaxf(m1[0], m1[1]), fmaxf(m1[2], m1[3])));
    }
    float p[4];
#pragma unroll
    for (int r = 0; r < 4; ++r) p[r] = __expf(sc[r] - mx);
    *(LAS u32x2*)(sm + L_P + n * 272 + (16 * w + 4 * g) * 2) = (u32x2){pk2(p[0], p[1]), pk2(p[2], p[3])};
    float ls = x32_sum(x16_sum((p[0] + p[1]) + (p[2] + p[3])));
    if (g == 0) *(LAS float*)(sm + L_LS + n * 32 + w * 4) = ls;
    bar();
    TS(7);
    f32x4 o[4] = {Z4, Z4, Z4, Z4};
#pragma unroll
    for (int ks = 0; ks < 4; ++ks) {
      u32x4 pf = *(const LAS u32x4*)(sm + L_P + n * 272 + (32 * ks + 8 * g) * 2);
#pragma unroll
      for (int t = 0; t < 4; ++t) o[t] = mfma(ca[4 * ks + t], pf, o[t]);
    }
    f32x4 l0 = *(const LAS f32x4*)(sm + L_LS + n * 32), l1 = *(const LAS f32x4*)(sm + L_LS + n * 32 + 16);
    const u32 rec = (u32)(((16 * cls + n) * 8 + w) * 128 + chunk);
    const u32x4 pa = {pk2(o[0][0], o[0][1]), pk2(o[0][2], o[0][3]), pk2(o[1][0], o[1][1]), pk2(o[1][2], o[1][3])};
    const u32x4 pb = {pk2(o[2][0], o[2][1]), pk2(o[2][2], o[2][3]), pk2(o[3][0], o[3][1]), pk2(o[3][2], o[3][3])};
    wst16(R, OFF_OP + rec * 128 + g * 32, pa);
    wst16(R, OFF_OP + rec * 128 + g * 32 + 16, pb);
    u32 rs = sum4(pa) + sum4(pb);
    { auto t = __builtin_amdgcn_permlane16_swap(rs, rs, false, false); rs = t[0] + t[1]; }
    { auto t = __builtin_amdgcn_permlane32_swap(rs, rs, false, false); rs = t[0] + t[1]; }
    const float lt = ((l0[0] + l0[1]) + (l0[2] + l0[3])) + ((l1[0] + l1[1]) + (l1[2] + l1[3]));
    const u32 um = __float_as_uint(mx), ul = __float_as_uint(lt);
    if (g == 0) wst16(R, OFF_ML + rec * 16, (u32x4){um, ul, rs + E, um + ul + E});
  }
  TS(8);
  if (cls == 1) {
    const u32 base = (u32)(h2 * 8 + 2 * cq) * 128;
    const u32 s0i = 16 * w + (l >> 3);
    const u32 oo = OFF_OP + (base + s0i) * 128 + (l & 7) * 16;
    const u32 ko = OFF_ML + (base + s0i) * 16;
    const u32 mo = OFF_ML + (base + 64 * (w >> 2) + l) * 16;
    u32x4 o[4], k[4];
    if (OP_SLEEP) __builtin_amdgcn_s_sleep(OP_SLEEP);
#pragma unroll
    for (int i = 0; i < 4; ++i) o[i] = wld16(R, oo + (i & 1) * 1024 + (i >> 1) * 16384);
    u32x4 ma = wld16(R, mo);
#pragma unroll
    for (int i = 0; i < 4; ++i) k[i] = wld16(R, ko + (i & 1) * 128 + (i >> 1) * 2048);
    sched_fence();
    u32x4 Fv[8], Fo[16];
    tr8x8(Wv, Fv);
#pragma unroll
    for (int ks = 0; ks < 4; ++ks) tr8x4(Wo + 8 * ks, Fo + 4 * ks);
#pragma unroll
    for (int i = 0; i < 16; ++i) pin(Fo[i]);
#pragma unroll
    for (int i = 0; i < 8; ++i) pin(Fv[i]);
    for (int it = 0; it < SPIN_MAX; ++it) {
      bool nb[4];
#pragma unroll
      for (int i = 0; i < 4; ++i) nb[i] = (sum8l(sum4(o[i])) + E != k[i][2]) | (k[i][0] + k[i][1] + E != k[i][3]);
      const bool n2 = ma[0] + ma[1] + E != ma[3];
      if (!__any(nb[0] | nb[1] | nb[2] | nb[3] | n2)) break;
      const u32 z = opq(0u);
#pragma unroll
      for (int i = 0; i < 4; ++i)
        if (nb[i]) { o[i] = wld16(R, oo + z + (i & 1) * 1024 + (i >> 1) * 16384); k[i] = wld16(R, ko + z + (i & 1) * 128 + (i >> 1) * 2048); }
      if (n2) ma = wld16(R, mo + z);
    }
    TS(9);
    const float mm = __uint_as_float(ma[0]);
    const float M = wave_max(mm);
    const float inv = rcp(wave_sum(__expf(mm - M) * __uint_as_float(ma[1])));
    const float w0 = __expf(__uint_as_float(k[0][0]) - M) * inv, w1 = __expf(__uint_as_float(k[1][0]) - M) * inv;
    float a[16];
#pragma unroll
    for (int i = 0; i < 4; ++i) {
      a[2 * i] = w0 * bflo(o[0][i]) + w1 * bflo(o[1][i]);
      a[2 * i + 1] = w0 * bfhi(o[0][i]) + w1 * bfhi(o[1][i]);
      a[8 + 2 * i] = w0 * bflo(o[2][i]) + w1 * bflo(o[3][i]);
      a[8 + 2 * i + 1] = w0 * bfhi(o[2][i]) + w1 * bfhi(o[3][i]);
    }
    float r8[8];
#pragma unroll
    for (int i = 0; i < 8; ++i) {
      auto sw = __builtin_amdgcn_permlane16_swap(__float_as_uint(a[i]), __float_as_uint(a[8 + i]), false, false);
      float t = __uint_as_float(sw[0]) + __uint_as_float(sw[1]);
      t += dpp<0x128>(t);
      r8[i] = x32_sum(t);
    }
    if (l < 32 && n < 8) {
      LAS f32x4* r = (LAS f32x4*)(sm + L_RED + w * 512 + g * 256 + n * 32);
      r[0] = (f32x4){r8[0], r8[1], r8[2], r8[3]}; r[1] = (f32x4){r8[4], r8[5], r8[6], r8[7]};
    }
    bar();
    if (w < 4) {
      const LAS f32x4* r = (const LAS f32x4*)(sm + L_RED + (n & 1) * 2048 + (w >> 1) * 256 + (16 * g + 8 * ((n >> 1) & 1)) * 4);
      f32x4 lo = (r[0] + r[32]) + (r[64] + r[96]), hi = (r[1] + r[33]) + (r[65] + r[97]);
      const u32x4 af = {pk2(lo[0], lo[1]), pk2(lo[2], lo[3]), pk2(hi[0], hi[1]), pk2(hi[2], hi[3])};
      float v0[8], v1[8];
#pragma unroll
      for (int i = 0; i < 8; ++i) {
        f32x4 c = mfma(af, Fv[i], Z4);
        float a0 = kg ? c[2] : c[0], a1 = kg ? c[3] : c[1];
        v0[i] = a0 + dpp<0x128>(a0);
        v1[i] = a1 + dpp<0x128>(a1);
      }
      if (l < 8) {
        LAS char* d = sm + L_HB + (w >> 1) * 512 + (w & 1) * 128 + l * 16;
        *(LAS u32x4*)d = (u32x4){pk2(v0[0], v0[1]), pk2(v0[2], v0[3]), pk2(v0[4], v0[5]), pk2(v0[6], v0[7])};
        *(LAS u32x4*)(d + 256) = (u32x4){pk2(v1[0], v1[1]), pk2(v1[2], v1[3]), pk2(v1[4], v1[5]), pk2(v1[6], v1[7])};
      }
    }
    bar();
    {
      f32x4 acc[4] = {Z4, Z4, Z4, Z4};
#pragma unroll
      for (int ks = 0; ks < 4; ++ks) {
        u32x4 ca = *(const LAS u32x4*)(sm + L_HB + (n & 3) * 256 + ks * 64 + g * 16);
#pragma unroll
        for (int i = 0; i < 4; ++i) acc[i] = mfma(ca, Fo[4 * ks + i], acc[i]);
      }
      u32x4 ov;
#pragma unroll
      for (int i = 0; i < 4; ++i) ov[i] = tg8((g & 1) ? acc[i][1] + acc[i][3] : acc[i][0] + acc[i][2], t8);
      if (g < 2) wst16(R, OFF_OUTP + (u32)(g * 16 + 2 * w + (n >> 3)) * 16384 + chunk * 128 + (n & 7) * 16, ov);
    }
    TS(10);
  } else if (chunk < 32) {
    const int b = chunk >> 4, m = (chunk & 15) * 32 + (l & 31);
    const u32 oo = OFF_OUTP + (u32)chunk * 16384 + w * 2048 + l * 16;
    const u32 go = OFF_MODG + (u32)xcd * 16384 + b * 2048 + m * 4;
    u32 ga = wld4(R, go), gb = wld4(R, go + 12288);
    const float xx = bflo(gld2(x + b * 512 + m));
    for (int it = 0; it < SPIN_MAX; ++it) {
      if (!__any((((ga ^ E) | (gb ^ E)) & 0xFFFFu) != 0u)) break;
      const u32 o = opq(go);
      ga = wld4(R, o); gb = wld4(R, o + 12288);
    }
    u32x4 v0 = wld16(R, oo), v1 = wld16(R, oo + 1024);
    for (int it = 0; it < SPIN_MAX; ++it) {
      const bool n0 = bad8(v0, E), n1 = bad8(v1, E);
      if (!__any(n0 | n1)) break;
      __builtin_amdgcn_s_sleep(1);
      const u32 o = opq(oo);
      if (n0) v0 = wld16(R, o);
      if (n1) v1 = wld16(R, o + 1024);
    }
    TS(11);
    f32x4 s4;
#pragma unroll
    for (int i = 0; i < 4; ++i) { float t = un8(v0[i]) + un8(v1[i]); t += dpp<0x128>(t); s4[i] = x32_sum(x16_sum(t)); }
    if (l < 8) *(LAS f32x4*)(sm + L_RED + w * 128 + l * 16) = s4;
    bar();
    const LAS float* r = (const LAS float*)(sm + L_RED + (l & 31) * 4);
    const float at = rb(((r[0] + r[32]) + (r[64] + r[96])) + ((r[128] + r[160]) + (r[192] + r[224])));
    const float x1 = rb(xx + rb(bfhi(ga) * at));
    if (l < 32) wst4(R, OFF_X1 + (u32)w * 4096 + b * 2048 + m * 4, tg16(x1, t16));
    if (chunk == 0 && tid == 0) wst4(R, 0, E);
    TS(12);
    const u32 fo = OFF_FIN + (u32)chunk * 8192 + w * 896 + (l < 56 ? l : l - 56) * 16;
    u32x4 f = wld16(R, fo);
    for (int it = 0; it < SPIN_MAX; ++it) {
      if (!__any(bad8(f, E))) break;
      __builtin_amdgcn_s_sleep(1);
      f = wld16(R, opq(fo));
    }
    TS(13);
#pragma unroll
    for (int i = 0; i < 4; ++i) { float t = l < 56 ? un8(f[i]) : 0.f; t += dpp<0x128>(t); s4[i] = x32_sum(x16_sum(t)); }
    if (l < 8) *(LAS f32x4*)(sm + L_RED + 1024 + w * 128 + l * 16) = s4;
    bar();
    if (w == 0) {
      const float y = rb(((r[256] + r[288]) + (r[320] + r[352])) + ((r[384] + r[416]) + (r[448] + r[480])));
      const float o = x1 + rb(bfhi(gb) * y);
      if (l < 32) *(GAS u16*)(out + b * 512 + m) = (u16)(pk2(o, o) & 0xFFFFu);
    }
    TS(14);
  } else if (fi >= 0) {
    u32x4 W[8], FG[8];
    {
      const u16* p = (colblk < 4 ? wg : wu) + (size_t)(64 * w + 32 * kg + 8 * g) * 1792 + fi * 32 + 8 * (colblk & 3);
#pragma unroll
      for (int i = 0; i < 8; ++i) W[i] = gld16(p + i * 1792);
    }
    u32x2 Wd[8];
    {
      const u16* p = wd + (size_t)(fi * 32 + 8 * g) * 512 + 64 * w + 4 * n;
#pragma unroll
      for (int i = 0; i < 8; ++i) Wd[i] = gl8<NT_W1>(p + i * 512);
    }
    sched_fence();
    u32x4 Fd[4];
    tr8x8(W, FG);
    tr8x4(Wd, Fd);
#pragma unroll
    for (int i = 0; i < 8; ++i) pin(FG[i]);
#pragma unroll
    for (int i = 0; i < 4; ++i) pin(Fd[i]);
    TS(10);
    if (w < 2) {
      const u32 so = OFF_MODG + (u32)xcd * 16384 + 4096 + (u32)w * 2048 + l * 16;
      u32x4 sb0 = wld16(R, so), sb1 = wld16(R, so + 1024), cb0 = wld16(R, so + 4096), cb1 = wld16(R, so + 5120);
      for (int it = 0; it < SPIN_MAX; ++it) {
        if (!__any(bad16(sb0, E) | bad16(sb1, E) | bad16(cb0, E) | bad16(cb1, E))) break;
        const u32 o = opq(so);
        sb0 = wld16(R, o); sb1 = wld16(R, o + 1024); cb0 = wld16(R, o + 4096); cb1 = wld16(R, o + 5120);
      }
      float c1[8], sh[8];
#pragma unroll
      for (int i = 0; i < 4; ++i) {
        c1[i] = rb(1.f + bfhi(cb0[i])); c1[4 + i] = rb(1.f + bfhi(cb1[i]));
        sh[i] = bfhi(sb0[i]); sh[4 + i] = bfhi(sb1[i]);
      }
#pragma unroll
      for (int i = 0; i < 8; ++i) { pin(c1[i]); pin(sh[i]); }
      TS(11);
      const u32 xo = OFF_X1 + (u32)xcd * 4096 + (u32)w * 2048 + l * 16;
      u32x4 a0 = wld16(R, xo), a1 = wld16(R, xo + 1024);
      for (int it = 0; it < SPIN_MAX; ++it) {
        if (!__any(bad16(a0, E) | bad16(a1, E))) break;
        const u32 o = opq(xo);
        a0 = wld16(R, o); a1 = wld16(R, o + 1024);
      }
      TS(12);
      float xv[8];
#pragma unroll
      for (int i = 0; i < 4; ++i) { xv[i] = bfhi(a0[i]); xv[4 + i] = bfhi(a1[i]); }
      const float m = wave_sum(((xv[0] + xv[1]) + (xv[2] + xv[3])) + ((xv[4] + xv[5]) + (xv[6] + xv[7]))) * (1.f / 512.f);
      float qq = 0.f;
#pragma unroll
      for (int i = 0; i < 8; ++i) { float d = xv[i] - m; qq += d * d; }
      const float var = wave_sum(qq) * (1.f / 512.f);
      const float mean = rb(m), sd = rcp(rb(__builtin_amdgcn_sqrtf(rb(rb(var) + 1e-5f))));
      float hv[8];
#pragma unroll
      for (int i = 0; i < 8; ++i) hv[i] = rb(rb(rb(xv[i] - mean) * sd) * c1[i]) + sh[i];
      *(LAS u32x2*)(sm + L_HB + w * 1024 + l * 8) = (u32x2){pk2(hv[0], hv[1]), pk2(hv[2], hv[3])};
      *(LAS u32x2*)(sm + L_HB + w * 1024 + 512 + l * 8) = (u32x2){pk2(hv[4], hv[5]), pk2(hv[6], hv[7])};
    }
    bar();
    {
      u32x4 af = *(const LAS u32x4*)(sm + L_HB + (n & 1) * 1024 + (64 * w + 32 * ((n >> 1) & 1) + 8 * g) * 2);
      float v0[8], v1[8];
#pragma unroll
      for (int i = 0; i < 8; ++i) {
        f32x4 a = mfma(af, FG[i], Z4);
        float a0 = kg ? a[2] : a[0], a1 = kg ? a[3] : a[1];
        v0[i] = a0 + dpp<0x128>(a0);
        v1[i] = a1 + dpp<0x128>(a1);
      }
      if (l < 8) {
        LAS f32x4* r = (LAS f32x4*)(sm + L_RED + 4096 + w * 512 + l * 32);
        r[0] = (f32x4){v0[0], v0[1], v0[2], v0[3]}; r[1] = (f32x4){v0[4], v0[5], v0[6], v0[7]};
        r[16] = (f32x4){v1[0], v1[1], v1[2], v1[3]}; r[17] = (f32x4){v1[4], v1[5], v1[6], v1[7]};
      }
    }
    bar();
    {
      const LAS float* r = (const LAS float*)(sm + L_RED + 4096 + (l >> 5) * 256 + (l & 31) * 4);
      float gs = ((r[0] + r[128]) + (r[256] + r[384])) + ((r[512] + r[640]) + (r[768] + r[896]));
      float us = ((r[32] + r[160]) + (r[288] + r[416])) + ((r[544] + r[672]) + (r[800] + r[928]));
      float hv = rb(gelu_f(rb(gs))) * rb(us);
      *(LAS u16*)(sm + L_P + w * 128 + l * 2) = (u16)(pk2(hv, hv) & 0xFFFFu);
      lds_wait();
      u32x4 ha = *(const LAS u32x4*)(sm + L_P + w * 128 + (n & 1) * 64 + g * 16);
      u32x4 o;
#pragma unroll
      for (int i = 0; i < 4; ++i) {
        f32x4 a = mfma(ha, Fd[i], Z4);
        o[i] = tg8((g & 1) ? a[1] : a[0], t8);
      }
      if (g < 2) wst16(R, OFF_FIN + (u32)(g * 16 + 2 * w + (n >> 3)) * 8192 + fi * 128 + (n & 7) * 16, o);
    }
    TS(13);
  }
}

extern "C" void kernel_launch(void* const* d_in, const int* in_sizes, int n_in, void* d_out, int out_size, void* d_ws,
                              size_t ws_size, hipStream_t stream) {
  (void)in_sizes; (void)n_in; (void)out_size; (void)ws_size;
  (void)hipFuncSetAttribute((const void*)fused_kernel, hipFuncAttributeMaxDynamicSharedMemorySize, (int)LDS_BYTES);
  hipLaunchKernelGGL(fused_kernel, dim3(256), dim3(512), LDS_BYTES, stream, (const u16*)d_in[0], (const u16*)d_in[1],
                     (const u16*)d_in[2], (const u16*)d_in[3], (const u16*)d_in[4], (const u16*)d_in[5], (const u16*)d_in[7],
                     (const u16*)d_in[8], (const u16*)d_in[9], (const u16*)d_in[10], (const u16*)d_in[11], (u16*)d_out, (char*)d_ws);
}
```

```cpp
#include <hip/hip_runtime.h>
#include <hip/hip_bf16.h>
#include <stdint.h>
#include <type_traits>

typedef unsigned short u16;
typedef unsigned int u32;
typedef __attribute__((ext_vector_type(4))) u32 u32x4;
typedef __attribute__((ext_vector_type(2))) u32 u32x2;
typedef __attribute__((ext_vector_type(4))) float f32x4;
typedef __attribute__((ext_vector_type(2))) float f32x2;
typedef __attribute__((ext_vector_type(8))) __bf16 bf16x8;
typedef __attribute__((ext_vector_type(2))) __bf16 bf16x2;
typedef __attribute__((ext_vector_type(4))) short s16x4;
#define LAS __attribute__((address_space(3)))
#define GAS __attribute__((address_space(1)))
#define DI __device__ __forceinline__

constexpr u32 OFF_HINT = 4096;
constexpr u32 OFF_HM = OFF_HINT + 16384;
constexpr u32 OFF_MODG = OFF_HM + 65536;
constexpr u32 OFF_X1 = OFF_MODG + 131072;
constexpr u32 OFF_QA = OFF_X1 + 32768;
constexpr u32 OFF_QK = OFF_QA + 131072;
constexpr u32 OFF_OP = OFF_QK + 65536;
constexpr u32 OFF_ML = OFF_OP + 4194304;
constexpr u32 OFF_OUTP = OFF_ML + 524288;
constexpr u32 OFF_FIN = OFF_OUTP + 524288;
constexpr u32 WS_BYTES = OFF_FIN + 262144;
constexpr u32 OFF_DBG = 8u << 20;
static_assert(WS_BYTES <= OFF_DBG, "ws");
constexpr int SPIN_MAX = 1 << 16;

constexpr u32 L_C = 0;
constexpr u32 L_Q = 0;
constexpr u32 L_P = 32768;
constexpr u32 L_MX = L_P + 8704;
constexpr u32 L_LS = L_MX + 1024;
constexpr u32 L_RED = 131072;
constexpr u32 L_HB = L_RED + 8192;
constexpr u32 L_QB = L_HB + 2048;
constexpr u32 LDS_BYTES = L_QB + 512;
static_assert(LDS_BYTES <= 163840, "lds");

DI float bflo(u32 v) { return __builtin_bit_cast(float, v << 16); }
DI float bfhi(u32 v) { return __builtin_bit_cast(float, v & 0xFFFF0000u); }
DI u32 pk2(float a, float b) {
  f32x2 f = {a, b};
  return __builtin_bit_cast(u32, __builtin_convertvector(f, bf16x2));
}
DI float rb(float a) { return bflo(pk2(a, a)); }
DI f32x4 mfma(u32x4 a, u32x4 b, f32x4 c) {
  return __builtin_amdgcn_mfma_f32_16x16x32_bf16(__builtin_bit_cast(bf16x8, a), __builtin_bit_cast(bf16x8, b), c, 0, 0, 0);
}
template <int CTRL> DI float dpp(float x) {
  return __builtin_bit_cast(float, __builtin_amdgcn_update_dpp(0, __builtin_bit_cast(int, x), CTRL, 0xF, 0xF, true));
}
DI float x16_sum(float x) { auto s = __builtin_amdgcn_permlane16_swap(__float_as_uint(x), __float_as_uint(x), false, false); return __uint_as_float(s[0]) + __uint_as_float(s[1]); }
DI float x32_sum(float x) { auto s = __builtin_amdgcn_permlane32_swap(__float_as_uint(x), __float_as_uint(x), false, false); return __uint_as_float(s[0]) + __uint_as_float(s[1]); }
DI float x16_max(float x) { auto s = __builtin_amdgcn_permlane16_swap(__float_as_uint(x), __float_as_uint(x), false, false); return fmaxf(__uint_as_float(s[0]), __uint_as_float(s[1])); }
DI float x32_max(float x) { auto s = __builtin_amdgcn_permlane32_swap(__float_as_uint(x), __float_as_uint(x), false, false); return fmaxf(__uint_as_float(s[0]), __uint_as_float(s[1])); }
DI float wave_sum(float x) {
  x += dpp<0xB1>(x); x += dpp<0x4E>(x); x += dpp<0x141>(x); x += dpp<0x140>(x);
  return x32_sum(x16_sum(x));
}
DI float wave_max(float x) {
  x = fmaxf(x, dpp<0xB1>(x)); x = fmaxf(x, dpp<0x4E>(x)); x = fmaxf(x, dpp<0x141>(x)); x = fmaxf(x, dpp<0x140>(x));
  return x32_max(x16_max(x));
}
DI float silu_f(float x) { return x * __builtin_amdgcn_rcpf(1.f + __expf(-x)); }
DI float gelu_f(float g) {
  float z = 0.7978845608028654f * (g + 0.044715f * g * g * g);
  float e = __expf(2.f * z);
  float t = 1.f - 2.f * __builtin_amdgcn_rcpf(e + 1.f);
  return 0.5f * g * (1.f + t);
}
DI u32x4 gld16(const void* p) { return *(const GAS u32x4*)p; }
DI u32x2 gld8(const void* p) { return *(const GAS u32x2*)p; }
DI u32 gld2(const void* p) { return *(const GAS u16*)p; }
typedef __amdgpu_buffer_rsrc_t rsrc_t;
DI u32x4 wld16(rsrc_t r, u32 off) { return __builtin_amdgcn_raw_buffer_load_b128(r, off, 0, 16); }
DI u32x2 wld8(rsrc_t r, u32 off) { return __builtin_amdgcn_raw_buffer_load_b64(r, off, 0, 16); }
DI u32 wld4(rsrc_t r, u32 off) { return __builtin_amdgcn_raw_buffer_load_b32(r, off, 0, 16); }
DI void wst16(rsrc_t r, u32 off, u32x4 v) { __builtin_amdgcn_raw_buffer_store_b128(v, r, off, 0, 16); }
DI void wst8(rsrc_t r, u32 off, u32x2 v) { __builtin_amdgcn_raw_buffer_store_b64(v, r, off, 0, 16); }
DI void wst4(rsrc_t r, u32 off, u32 v) { __builtin_amdgcn_raw_buffer_store_b32(v, r, off, 0, 16); }
DI u32 opq(u32 v) { asm volatile("" : "+v"(v)); return v; }
template <class T> DI void pin(T& v) { asm volatile("" : "+v"(v)); }
DI float rcp(float x) { return __builtin_amdgcn_rcpf(x); }
DI void fence_c() { asm volatile("" ::: "memory"); }
DI void sched_fence() { __builtin_amdgcn_sched_barrier(0); }
DI void lds_wait() { asm volatile("s_waitcnt lgkmcnt(0)" ::: "memory"); }
DI void bar() { asm volatile("s_waitcnt lgkmcnt(0)\n\ts_barrier" ::: "memory"); }
DI void tr8x8(const u32x4 (&W)[8], u32x4 (&F)[8]) {
#pragma unroll
  for (int i = 0; i < 8; ++i)
#pragma unroll
    for (int p = 0; p < 4; ++p)
      F[i][p] = __builtin_amdgcn_perm(W[2 * p + 1][i >> 1], W[2 * p][i >> 1], (i & 1) ? 0x07060302u : 0x05040100u);
}
DI void tr8x4(const u32x2* W, u32x4* F) {
#pragma unroll
  for (int i = 0; i < 4; ++i)
#pragma unroll
    for (int p = 0; p < 4; ++p)
      F[i][p] = __builtin_amdgcn_perm(W[2 * p + 1][i >> 1], W[2 * p][i >> 1], (i & 1) ? 0x07060302u : 0x05040100u);
}
DI void ln_stats(u32x4 v, float& mean, float& sd) {
  float s = 0.f;
#pragma unroll
  for (int i = 0; i < 4; ++i) s += bflo(v[i]) + bfhi(v[i]);
  float m = wave_sum(s) * (1.f / 512.f);
  float q = 0.f;
#pragma unroll
  for (int i = 0; i < 4; ++i) { float a = bflo(v[i]) - m, b = bfhi(v[i]) - m; q += a * a + b * b; }
  float var = wave_sum(q) * (1.f / 512.f);
  mean = rb(m);
  sd = __builtin_amdgcn_rcpf(rb(__builtin_amdgcn_sqrtf(rb(rb(var) + 1e-5f))));
}
DI float ln_mod(float x, float mean, float sd, float sc, float sh) {
  float ln = rb(rb(x - mean) * sd);
  return rb(rb(ln * rb(1.f + sc)) + sh);
}

#define NT_WQ 1
#define NT_CB 1
#define NT_CA 0
#define NT_W1 1
#define HINT_SLEEP 40
#define B_DELAY 60
#define OP_SLEEP 0
#define B_GATE 0
#define A_EARLY 0
#define ROT 0
#define OP_TH 64
#define OP_PS 4
#define WO_EARLY 1
#define NT_WO 1
template <int NT> DI u32x4 gl16(const void* p) {
  if (NT) return __builtin_nontemporal_load((const GAS u32x4*)p);
  return *(const GAS u32x4*)p;
}
template <int NT> DI u32x2 gl8(const void* p) {
  if (NT) return __builtin_nontemporal_load((const GAS u32x2*)p);
  return *(const GAS u32x2*)p;
}
template <int NT> DI void dma16(const void* gp, u32 lds) {
  u32 keep;
  if (NT) asm volatile("s_mov_b32 %0, m0\n\ts_mov_b32 m0, %2\n\ts_nop 0\n\tglobal_load_lds_dwordx4 %1, off nt\n\ts_mov_b32 m0, %0" : "=&s"(keep) : "v"(gp), "s"(lds) : "memory");
  else asm volatile("s_mov_b32 %0, m0\n\ts_mov_b32 m0, %2\n\ts_nop 0\n\tglobal_load_lds_dwordx4 %1, off\n\ts_mov_b32 m0, %0" : "=&s"(keep) : "v"(gp), "s"(lds) : "memory");
}
template <int CTRL> DI u32 dppu(u32 x) { return (u32)__builtin_amdgcn_update_dpp(0, (int)x, CTRL, 0xF, 0xF, true); }
DI u32 sum8l(u32 x) { x += dppu<0xB1>(x); x += dppu<0x4E>(x); x += dppu<0x141>(x); return x; }
DI u32 sum16l(u32 x) { x = sum8l(x); x += dppu<0x140>(x); return x; }
DI bool bad16x2(u32x2 v, u32 t) { return (((v[0] ^ t) | (v[1] ^ t)) & 0xFFFFu) != 0u; }
DI void vm_wait0() { asm volatile("s_waitcnt vmcnt(0)" ::: "memory"); }
DI u32 sum4(u32x4 v) { return (v[0] + v[1]) + (v[2] + v[3]); }
DI bool bad16(u32x4 v, u32 t) { return (((v[0] ^ t) | (v[1] ^ t) | (v[2] ^ t) | (v[3] ^ t)) & 0xFFFFu) != 0u; }
DI bool bad8(u32x4 v, u32 t) { return (((v[0] ^ t) | (v[1] ^ t) | (v[2] ^ t) | (v[3] ^ t)) & 0xFFu) != 0u; }
DI u32 tg16(float bfval, u32 t16) { return (__float_as_uint(bfval) & 0xFFFF0000u) | t16; }
DI u32 tg8(float v, u32 t8) { return (__float_as_uint(v) & 0xFFFFFF00u) | t8; }
DI float un8(u32 v) { return __uint_as_float(v & 0xFFFFFF00u); }

#define TS(i) do {} while (0)

__global__ void __launch_bounds__(512, 2)
fused_kernel(const u16* __restrict__ x, const u16* __restrict__ ckv, const u16* __restrict__ wq, const u16* __restrict__ wuk,
             const u16* __restrict__ wuv, const u16* __restrict__ wo, const u16* __restrict__ wg, const u16* __restrict__ wu,
             const u16* __restrict__ wd, const u16* __restrict__ cnd, const u16* __restrict__ wmod, u16* __restrict__ out,
             char* ws) {
  extern __shared__ __attribute__((aligned(16))) char smem_raw[];
  LAS char* sm = (LAS char*)smem_raw;
  const u32 lds0 = (u32)(size_t)sm;
  const int tid = threadIdx.x, l = tid & 63;
  const int w = __builtin_amdgcn_readfirstlane(tid >> 6);
  const int n = l & 15, g = l >> 4;
  const int bid = blockIdx.x, xcd = bid & 7, j = bid >> 3;
  const int cls = (j >= 2 && j < 18) ? 0 : 1, pairidx = j < 2 ? j : (j < 18 ? j - 2 : j - 16);
  const int chunk = xcd + 8 * pairidx;
  const int h2 = 4 * xcd + (pairidx >> 2), cq = pairidx & 3;
  const int fi = chunk - 72;
  const rsrc_t R = __builtin_amdgcn_make_buffer_rsrc(ws, 0, WS_BYTES, 0x00020000);
  const f32x4 Z4 = {0.f, 0.f, 0.f, 0.f};
  TS(0);
  const u32 Ev = *(const GAS u32*)ws + 1u;
  const int colblk = n & 7, kg = n >> 3;
  u32x4 Wv[8];
  const int rot = (pairidx + 2 * w) & 15;
  auto load_rows = [&](const u16* src, int r0, auto nr, auto nt, int ib = 0) {
#pragma unroll
    for (int i = 0; i < decltype(nr)::value; ++i) {
      if (ROT && decltype(nr)::value == 16) {
        const int ii = (i + rot) & 15;
        const u32 swz = ((ii & 3) << 2) | (ii >> 2);
        dma16<decltype(nt)::value>(src + (size_t)(r0 + ii) * 512 + (l ^ swz) * 8, lds0 + L_C + (r0 + ii) * 1024);
      } else {
        const u32 swz = (((i + ib) & 3) << 2) | (((i + ib) >> 2) & 3);
        dma16<decltype(nt)::value>(src + (size_t)(r0 + i) * 512 + (l ^ swz) * 8, lds0 + L_C + (r0 + i) * 1024);
      }
    }
  };
  auto read_tr = [&](u32x4 (&f)[16]) {
    const int q = n >> 2, pp = n & 3;
#pragma unroll
    for (int ks = 0; ks < 4; ++ks)
#pragma unroll
      for (int t = 0; t < 4; ++t)
#pragma unroll
        for (int tt = 0; tt < 2; ++tt) {
          const int row = 32 * ks + 8 * g + 4 * tt + q;
          const u32 swz = (q << 2) | ((2 * g + tt) & 3);
          const u32 ad = L_C + row * 1024 + (((8 * w + 2 * t + (pp >> 1)) ^ swz) * 16) + 8 * (pp & 1);
          u32x2 v = __builtin_bit_cast(u32x2, __builtin_amdgcn_ds_read_tr16_b64_v4i16((LAS s16x4*)(sm + ad)));
          f[4 * ks + t][2 * tt] = v[0]; f[4 * ks + t][2 * tt + 1] = v[1];
        }
  };
  auto mod_cols = [&](int cb) -> u32 {
    u32x4 cfr = gld16(cnd + (n & 1) * 512 + 64 * w + 32 * ((n >> 1) & 1) + 8 * g);
    u32x4 W[8];
    const u16* p = wmod + (size_t)(64 * w + 32 * kg + 8 * g) * 3072 + cb * 64 + 8 * colblk;
#pragma unroll
    for (int i = 0; i < 8; ++i) W[i] = gl16<NT_W1>(p + i * 3072);
    sched_fence();
    u32x4 af;
#pragma unroll
    for (int i = 0; i < 4; ++i) af[i] = pk2(silu_f(bflo(cfr[i])), silu_f(bfhi(cfr[i])));
    pin(af);
    u32x4 F[8];
    tr8x8(W, F);
    float v0[8], v1[8];
#pragma unroll
    for (int i = 0; i < 8; ++i) {
      f32x4 a = mfma(af, F[i], Z4);
      float a0 = kg ? a[2] : a[0], a1 = kg ? a[3] : a[1];
      v0[i] = a0 + dpp<0x128>(a0);
      v1[i] = a1 + dpp<0x128>(a1);
    }
    if (l < 8) {
      LAS f32x4* r = (LAS f32x4*)(sm + L_RED + w * 512 + l * 32);
      r[0] = (f32x4){v0[0], v0[1], v0[2], v0[3]}; r[1] = (f32x4){v0[4], v0[5], v0[6], v0[7]};
      r[16] = (f32x4){v1[0], v1[1], v1[2], v1[3]}; r[17] = (f32x4){v1[4], v1[5], v1[6], v1[7]};
    }
    bar();
    const LAS float* r = (const LAS float*)(sm + L_RED + l * 4);
    float m0 = ((r[0] + r[128]) + (r[256] + r[384])) + ((r[512] + r[640]) + (r[768] + r[896]));
    float m1 = ((r[64] + r[192]) + (r[320] + r[448])) + ((r[576] + r[704]) + (r[832] + r[960]));
    return pk2(m0, m1);
  };
  typedef std::integral_constant<int, 16> I16;
  typedef std::integral_constant<int, 32> I32;
  const u16* crow = ckv + (size_t)chunk * 128 * 512;
  const u16* worow = wo + (size_t)h2 * 128 * 512;
  u32x4 cf[16], ca[16];
  auto read_frags = [&]() {
    const u32 swz = ((n & 3) << 2) | (n >> 2);
#pragma unroll
    for (int ks = 0; ks < 16; ++ks) cf[ks] = *(const LAS u32x4*)(sm + L_C + (16 * w + n) * 1024 + (((4 * ks + g) ^ swz) * 16));
    read_tr(ca);
#pragma unroll
    for (int ks = 0; ks < 16; ++ks) { pin(cf[ks]); pin(ca[ks]); }
  };
  auto load_s4 = [&]() {
    if (w < 4) {
      const u16* p = wuv + (size_t)(cq * 128 + (w >> 1) * 64 + 32 * kg + 4 * g) * 4096 + h2 * 128 + 64 * (w & 1) + 8 * colblk;
#pragma unroll
      for (int i = 0; i < 8; ++i) Wv[i] = gl16<NT_W1>(p + (size_t)(16 * (i >> 2) + (i & 3)) * 4096);
    }
    load_rows(worow, 16 * w, I16(), std::integral_constant<int, NT_WO>());
  };
  auto hint_wait = [&](u32 e) {
    if (w == 0) {
      const u32 ho = OFF_HINT + (u32)xcd * 2048 + n * 128;
      for (int it = 0; it < SPIN_MAX; ++it) {
        if (!__any(wld4(R, opq(ho)) != e)) break;
        __builtin_amdgcn_s_sleep(2);
      }
      if (HINT_SLEEP) __builtin_amdgcn_s_sleep(HINT_SLEEP);
    }
    bar();
  };
  const u32 rb_ = (u32)(xcd & 1) * 2 + (chunk >> 6);
  u32 E;

  if (cls == 0) {
    u32x4 xv0 = gld16(x + 8 * l), xv1 = gld16(x + 512 + 8 * l);
    const u32 xm0 = *(const GAS u32*)(x + 2 * (tid & 255)), xm1 = *(const GAS u32*)(x + 512 + 2 * (tid & 255));
    u32x4 W[16];
    {
      const u16* p = wq + (size_t)(64 * w + 8 * g) * 4096 + h2 * 128 + 8 * n;
#pragma unroll
      for (int i = 0; i < 16; ++i) W[i] = gl16<NT_WQ>(p + (size_t)(32 * (i >> 3) + (i & 7)) * 4096);
    }
#pragma unroll
    for (int i = 0; i < 4; ++i)
      dma16<NT_W1>(wuk + (size_t)(128 * cq + 16 * w + 4 * i + g) * 4096 + h2 * 128 + (n ^ (4 * i + g)) * 8, lds0 + L_C + w * 16384 + 12288 + i * 1024);
    sched_fence();
    E = __builtin_amdgcn_readfirstlane(Ev);
    float mean0, sd0, mean1, sd1;
    ln_stats(xv0, mean0, sd0);
    ln_stats(xv1, mean1, sd1);
    float ln00 = rb(rb(bflo(xm0) - mean0) * sd0), ln01 = rb(rb(bflo(xm1) - mean1) * sd1);
    float ln10 = rb(rb(bfhi(xm0) - mean0) * sd0), ln11 = rb(rb(bfhi(xm1) - mean1) * sd1);
    u32x4 F[16];
    tr8x8(*(const u32x4(*)[8])(W), *(u32x4(*)[8])(F));
    tr8x8(*(const u32x4(*)[8])(W + 8), *(u32x4(*)[8])(F + 8));
#pragma unroll
    for (int i = 0; i < 16; ++i) pin(F[i]);
    pin(ln00); pin(ln01); pin(ln10); pin(ln11);
    vm_wait0();
    u32x4 uk[4];
#pragma unroll
    for (int ks = 0; ks < 4; ++ks) uk[ks] = *(const LAS u32x4*)(sm + L_C + w * 16384 + 12288 + n * 256 + (((4 * ks + g) ^ n) * 16));
#pragma unroll
    for (int i = 0; i < 4; ++i) pin(uk[i]);
    TS(1);
    if (w < 4) {
      const u32 ho = OFF_HM + (u32)xcd * 8192 + tid * 16;
      u32x4 hs = wld16(R, ho), hc = wld16(R, ho + 4096);
      for (int it = 0; it < SPIN_MAX; ++it) {
        if (!__any(bad16(hs, E) | bad16(hc, E))) break;
        const u32 o = opq(ho);
        hs = wld16(R, o); hc = wld16(R, o + 4096);
      }
      const float h00 = rb(ln00 * rb(1.f + bfhi(hc[0]))) + bfhi(hs[0]), h01 = rb(ln01 * rb(1.f + bfhi(hc[1]))) + bfhi(hs[1]);
      const float h10 = rb(ln10 * rb(1.f + bfhi(hc[2]))) + bfhi(hs[2]), h11 = rb(ln11 * rb(1.f + bfhi(hc[3]))) + bfhi(hs[3]);
      *(LAS u32*)(sm + L_HB + tid * 4) = pk2(h00, h10);
      *(LAS u32*)(sm + L_HB + 1024 + tid * 4) = pk2(h01, h11);
    }
    TS(2);
    bar();
    {
      u32x4 a0 = *(const LAS u32x4*)(sm + L_HB + (n & 1) * 1024 + w * 128 + g * 16);
      u32x4 a1 = *(const LAS u32x4*)(sm + L_HB + (n & 1) * 1024 + w * 128 + 64 + g * 16);
      f32x4 acc[8];
#pragma unroll
      for (int i = 0; i < 8; ++i) acc[i] = mfma(a0, F[i], Z4);
#pragma unroll
      for (int i = 0; i < 8; ++i) acc[i] = mfma(a1, F[8 + i], acc[i]);
      if (g == 0) {
        LAS f32x4* r = (LAS f32x4*)(sm + L_RED + w * 1024 + n * 32);
        r[0] = (f32x4){acc[0][0], acc[1][0], acc[2][0], acc[3][0]}; r[1] = (f32x4){acc[4][0], acc[5][0], acc[6][0], acc[7][0]};
        r[32] = (f32x4){acc[0][1], acc[1][1], acc[2][1], acc[3][1]}; r[33] = (f32x4){acc[4][1], acc[5][1], acc[6][1], acc[7][1]};
      }
    }
    bar();
    if (w < 4) {
      const LAS float* r = (const LAS float*)(sm + L_RED + tid * 4);
      float s = ((r[0] + r[256]) + (r[512] + r[768])) + ((r[1024] + r[1280]) + (r[1536] + r[1792]));
      *(LAS u16*)(sm + L_QB + tid * 2) = (u16)(pk2(s, s) & 0xFFFFu);
    }
    bar();
    {
      u32x4 qb[4];
#pragma unroll
      for (int ks = 0; ks < 4; ++ks) qb[ks] = *(const LAS u32x4*)(sm + L_QB + (n & 1) * 256 + ks * 64 + g * 16);
      f32x4 d0 = mfma(uk[0], qb[0], Z4), d1 = mfma(uk[1], qb[1], Z4);
      d0 = mfma(uk[2], qb[2], d0); d1 = mfma(uk[3], qb[3], d1);
      d0 += d1;
      if (n < 2) *(LAS u32x2*)(sm + L_HB + n * 256 + w * 32 + g * 8) = (u32x2){pk2(d0[0], d0[1]), pk2(d0[2], d0[3])};
    }
    bar();
    if (w == 0) {
      const u32x4 pay = *(const LAS u32x4*)(sm + L_HB + (g & 1) * 256 + n * 16);
      wst16(R, OFF_QA + (u32)(g * 32 + h2) * 1024 + cq * 256 + n * 16, pay);
      const u32 ck = sum16l(sum4(pay)) + E;
      if (n < 8) wst16(R, OFF_QK + (u32)((g * 32 + h2) * 4 + cq) * 128 + n * 16, (u32x4){ck, ck, ck, ck});
    }
    TS(3);
    load_s4();
  } else {
    if (chunk < 16) {
      const u32 pm = mod_cols(chunk);
      TS(1);
      E = __builtin_amdgcn_readfirstlane(Ev);
      const u32 tt16 = E & 0xFFFFu;
      wst8(R, OFF_HM + (u32)w * 8192 + (chunk >> 3) * 4096 + ((chunk & 7) * 64 + l) * 8, (u32x2){(pm << 16) | tt16, (pm & 0xFFFF0000u) | tt16});
      if (l < 8) wst16(R, OFF_HINT + (u32)w * 2048 + chunk * 128 + l * 16, (u32x4){E, E, E, E});
      TS(2);
    } else if (B_GATE) {
      E = __builtin_amdgcn_readfirstlane(Ev);
      hint_wait(E);
    } else if (B_DELAY) __builtin_amdgcn_s_sleep(B_DELAY);
    load_rows(crow, 16 * w, I16(), std::integral_constant<int, NT_CB>());
    E = __builtin_amdgcn_readfirstlane(Ev);
    vm_wait0();
    bar();
    TS(3);
    read_frags();
    TS(4);
    if (!B_GATE || chunk < 16) hint_wait(E); else bar();
    {
      const u32 qo = OFF_QA + (rb_ * 32 + 4 * w) * 1024 + l * 16, ko = OFF_QK + ((rb_ * 32 + 4 * w + (n & 3)) * 4 + g) * 128;
      u32x4 q[4];
#pragma unroll
      for (int i = 0; i < 4; ++i) q[i] = wld16(R, qo + i * 1024);
      u32 qk = wld4(R, ko);
      for (int it = 0; it < SPIN_MAX; ++it) {
        const u32 c0 = sum16l(sum4(q[0])), c1 = sum16l(sum4(q[1])), c2 = sum16l(sum4(q[2])), c3 = sum16l(sum4(q[3]));
        const u32 cs = (n & 2) ? ((n & 1) ? c3 : c2) : ((n & 1) ? c1 : c0);
        const unsigned long long bad = __ballot(cs + E != qk);
        if (!bad) break;
        const u32 z = opq(0u);
#pragma unroll
        for (int i = 0; i < 4; ++i)
          if (bad & (0x1111111111111111ull << i)) q[i] = wld16(R, qo + z + i * 1024);
        qk = wld4(R, ko + z);
      }
#pragma unroll
      for (int i = 0; i < 4; ++i) *(LAS u32x4*)(sm + L_Q + (4 * w + i) * 1024 + (l ^ ((i << 2) | (w & 3))) * 16) = q[i];
    }
    TS(5);
    sched_fence();
    bar();
    TS(6);
    f32x4 sa[2];
    {
      const u32 qswz = ((n & 3) << 2) | (n >> 2);
      u32x4 qf[2][8];
#pragma unroll
      for (int u = 0; u < 2; ++u)
#pragma unroll
        for (int ks = 0; ks < 8; ++ks) qf[u][ks] = *(const LAS u32x4*)(sm + L_Q + n * 1024 + ((4 * (8 * u + ks) + g) ^ qswz) * 16);
      sched_fence();
#pragma unroll
      for (int hh = 0; hh < 2; ++hh) {
        f32x4 s0 = Z4, s1 = Z4, s2 = Z4, s3 = Z4;
#pragma unroll
        for (int u = 0; u < 2; ++u) {
#pragma unroll
          for (int ks = 0; ks < 8; ks += 4) {
            s0 = mfma(cf[8 * u + ks], qf[u][ks], s0); s1 = mfma(cf[8 * u + ks + 1], qf[u][ks + 1], s1);
            s2 = mfma(cf[8 * u + ks + 2], qf[u][ks + 2], s2); s3 = mfma(cf[8 * u + ks + 3], qf[u][ks + 3], s3);
          }
          if (hh == 0) {
#pragma unroll
            for (int ks = 0; ks < 8; ++ks) qf[u][ks] = *(const LAS u32x4*)(sm + L_Q + (16 + n) * 1024 + ((4 * (8 * u + ks) + g) ^ qswz) * 16);
          }
          sched_fence();
        }
        sa[hh] = (s0 + s1) + (s2 + s3);
      }
    }
    float sc[2][4], mx[2];
#pragma unroll
    for (int hh = 0; hh < 2; ++hh) {
#pragma unroll
      for (int r = 0; r < 4; ++r) sc[hh][r] = rb(rb(sa[hh][r]) * 0.08838834764831845f);
      mx[hh] = x32_max(x16_max(fmaxf(fmaxf(sc[hh][0], sc[hh][1]), fmaxf(sc[hh][2], sc[hh][3]))));
    }
    if (g < 2) *(LAS float*)(sm + L_MX + (16 * g + n) * 32 + w * 4) = g ? mx[1] : mx[0];
    TS(9);
    bar();
    TS(15);
#pragma unroll
    for (int hh = 0; hh < 2; ++hh) {
      f32x4 m0 = *(const LAS f32x4*)(sm + L_MX + (16 * hh + n) * 32), m1 = *(const LAS f32x4*)(sm + L_MX + (16 * hh + n) * 32 + 16);
      mx[hh] = fmaxf(fmaxf(fmaxf(m0[0], m0[1]), fmaxf(m0[2], m0[3])), fmaxf(fmaxf(m1[0], m1[1]), fmaxf(m1[2], m1[3])));
    }
    float ls[2];
#pragma unroll
    for (int hh = 0; hh < 2; ++hh) {
      float p[4];
#pragma unroll
      for (int r = 0; r < 4; ++r) p[r] = __expf(sc[hh][r] - mx[hh]);
      *(LAS u32x2*)(sm + L_P + (16 * hh + n) * 272 + (16 * w + 4 * g) * 2) = (u32x2){pk2(p[0], p[1]), pk2(p[2], p[3])};
      ls[hh] = x32_sum(x16_sum((p[0] + p[1]) + (p[2] + p[3])));
    }
    if (g < 2) *(LAS float*)(sm + L_LS + (16 * g + n) * 32 + w * 4) = g ? ls[1] : ls[0];
    bar();
    TS(7);
#pragma unroll
    for (int hh = 0; hh < 2; ++hh) {
      f32x4 o[4] = {Z4, Z4, Z4, Z4};
#pragma unroll
      for (int ks = 0; ks < 4; ++ks) {
        u32x4 pf = *(const LAS u32x4*)(sm + L_P + (16 * hh + n) * 272 + (32 * ks + 8 * g) * 2);
#pragma unroll
        for (int t = 0; t < 4; ++t) o[t] = mfma(ca[4 * ks + t], pf, o[t]);
      }
      f32x4 l0 = *(const LAS f32x4*)(sm + L_LS + (16 * hh + n) * 32), l1 = *(const LAS f32x4*)(sm + L_LS + (16 * hh + n) * 32 + 16);
      const u32x4 pa = {pk2(o[0][0], o[0][1]), pk2(o[0][2], o[0][3]), pk2(o[1][0], o[1][1]), pk2(o[1][2], o[1][3])};
      const u32x4 pb = {pk2(o[2][0], o[2][1]), pk2(o[2][2], o[2][3]), pk2(o[3][0], o[3][1]), pk2(o[3][2], o[3][3])};
      u32x4 px;
#pragma unroll
      for (int i = 0; i < 4; ++i) px[i] = dppu<0x128>(pb[i]);
      const u32 r0 = OFF_OP + (u32)(((16 * hh + (n & 7)) * 8 + w) * 128 + chunk) * 128 + g * 32;
      wst16(R, r0 + (n & 8) * 2, (n & 8) ? px : pa);
      wst16(R, r0 + 8 * 8 * 128 * 128 + 16 - (n & 8) * 2, (n & 8) ? pa : px);
      u32 rs = sum4(pa) + sum4(pb);
      { auto t = __builtin_amdgcn_permlane16_swap(rs, rs, false, false); rs = t[0] + t[1]; }
      { auto t = __builtin_amdgcn_permlane32_swap(rs, rs, false, false); rs = t[0] + t[1]; }
      const float lt = ((l0[0] + l0[1]) + (l0[2] + l0[3])) + ((l1[0] + l1[1]) + (l1[2] + l1[3]));
      const u32 um = __float_as_uint(mx[hh]), ul = __float_as_uint(lt);
      if (g == 0) wst16(R, OFF_ML + (u32)((w * 128 + chunk) * 32 + 16 * hh + n) * 16, (u32x4){um, ul, rs + E, um + ul + E});
    }
    bar();
    TS(8);
  }
  const u32 t8 = E & 0xFFu, t16 = E & 0xFFFFu;
  if (cls == 0) {
    const u32 base = (u32)(h2 * 8 + 2 * cq) * 128;
    const u32 s0i = 16 * w + (l >> 3);
    const u32 oo = OFF_OP + (base + s0i) * 128 + (l & 7) * 16;
    const u32 kq = OFF_ML + ((2 * cq * 128 + s0i) * 32 + h2) * 16;
    u32x4 o[4], k[4];
    vm_wait0();
    bar();
    TS(4);
    u32x4 Fv[8], Fo[16];
    tr8x8(Wv, Fv);
    read_tr(Fo);
#pragma unroll
    for (int i = 0; i < 16; ++i) pin(Fo[i]);
#pragma unroll
    for (int i = 0; i < 8; ++i) pin(Fv[i]);
    TS(5);
    if (OP_TH) {
      if (w == 0) {
        const u32 go = OFF_ML + ((2 * cq * 128 + l) * 32 + h2) * 16;
        for (int it = 0; it < SPIN_MAX; ++it) {
          const u32 z = opq(go);
          const u32x4 g0 = wld16(R, z), g1 = wld16(R, z + 64 * 512);
          const int c = __builtin_popcountll(__ballot(g0[0] + g0[1] + E == g0[3])) + __builtin_popcountll(__ballot(g1[0] + g1[1] + E == g1[3]));
          if (c >= OP_TH) break;
          __builtin_amdgcn_s_sleep(OP_PS);
        }
      }
      bar();
    }
    TS(6);
#pragma unroll
    for (int i = 0; i < 4; ++i) o[i] = wld16(R, oo + (i & 1) * 1024 + (i >> 1) * 16384);
#pragma unroll
    for (int i = 0; i < 4; ++i) k[i] = wld16(R, kq + (i & 1) * 4096 + (i >> 1) * 65536);
    for (int it = 0; it < SPIN_MAX; ++it) {
      bool nb[4];
#pragma unroll
      for (int i = 0; i < 4; ++i) nb[i] = (sum8l(sum4(o[i])) + E != k[i][2]) | (k[i][0] + k[i][1] + E != k[i][3]);
      if (!__any(nb[0] | nb[1] | nb[2] | nb[3])) break;
      const u32 z = opq(0u);
#pragma unroll
      for (int i = 0; i < 4; ++i)
        if (nb[i]) { o[i] = wld16(R, oo + z + (i & 1) * 1024 + (i >> 1) * 16384); k[i] = wld16(R, kq + z + (i & 1) * 4096 + (i >> 1) * 65536); }
    }
    TS(9);
    float Mw = fmaxf(__uint_as_float(k[0][0]), __uint_as_float(k[1][0]));
    Mw = fmaxf(Mw, dpp<0x128>(Mw));
    Mw = x32_max(x16_max(Mw));
    const float w0 = __expf(__uint_as_float(k[0][0]) - Mw), w1 = __expf(__uint_as_float(k[1][0]) - Mw);
    float Lw = w0 * __uint_as_float(k[0][1]) + w1 * __uint_as_float(k[1][1]);
    Lw += dpp<0x128>(Lw);
    Lw = x32_sum(x16_sum(Lw));
    if (l == 0) *(LAS f32x2*)(sm + L_QB + w * 8) = (f32x2){Mw, Lw};
    float a[16];
#pragma unroll
    for (int i = 0; i < 4; ++i) {
      a[2 * i] = w0 * bflo(o[0][i]) + w1 * bflo(o[1][i]);
      a[2 * i + 1] = w0 * bfhi(o[0][i]) + w1 * bfhi(o[1][i]);
      a[8 + 2 * i] = w0 * bflo(o[2][i]) + w1 * bflo(o[3][i]);
      a[8 + 2 * i + 1] = w0 * bfhi(o[2][i]) + w1 * bfhi(o[3][i]);
    }
    float r8[8];
#pragma unroll
    for (int i = 0; i < 8; ++i) {
      auto sw = __builtin_amdgcn_permlane16_swap(__float_as_uint(a[i]), __float_as_uint(a[8 + i]), false, false);
      float t = __uint_as_float(sw[0]) + __uint_as_float(sw[1]);
      t += dpp<0x128>(t);
      r8[i] = x32_sum(t);
    }
    if (l < 32 && n < 8) {
      LAS f32x4* r = (LAS f32x4*)(sm + L_RED + w * 512 + g * 256 + n * 32);
      r[0] = (f32x4){r8[0], r8[1], r8[2], r8[3]}; r[1] = (f32x4){r8[4], r8[5], r8[6], r8[7]};
    }
    TS(11);
    bar();
    TS(12);
    if (w < 4) {
      const LAS f32x4* r = (const LAS f32x4*)(sm + L_RED + (n & 1) * 2048 + (w >> 1) * 256 + (16 * g + 8 * ((n >> 1) & 1)) * 4);
      const f32x4 e0 = *(const LAS f32x4*)(sm + L_QB + (n & 1) * 32), e1 = *(const LAS f32x4*)(sm + L_QB + (n & 1) * 32 + 16);
      const float M = fmaxf(fmaxf(e0[0], e0[2]), fmaxf(e1[0], e1[2]));
      float f0 = __expf(e0[0] - M), f1 = __expf(e0[2] - M), f2 = __expf(e1[0] - M), f3 = __expf(e1[2] - M);
      const float inv = rcp((f0 * e0[1] + f1 * e0[3]) + (f2 * e1[1] + f3 * e1[3]));
      f0 *= inv; f1 *= inv; f2 *= inv; f3 *= inv;
      f32x4 lo = (r[0] * f0 + r[32] * f1) + (r[64] * f2 + r[96] * f3), hi = (r[1] * f0 + r[33] * f1) + (r[65] * f2 + r[97] * f3);
      const u32x4 af = {pk2(lo[0], lo[1]), pk2(lo[2], lo[3]), pk2(hi[0], hi[1]), pk2(hi[2], hi[3])};
      float v0[8], v1[8];
#pragma unroll
      for (int i = 0; i < 8; ++i) {
        f32x4 c = mfma(af, Fv[i], Z4);
        float a0 = kg ? c[2] : c[0], a1 = kg ? c[3] : c[1];
        v0[i] = a0 + dpp<0x128>(a0);
        v1[i] = a1 + dpp<0x128>(a1);
      }
      if (l < 8) {
        LAS char* d = sm + L_HB + (w >> 1) * 512 + (w & 1) * 128 + l * 16;
        *(LAS u32x4*)d = (u32x4){pk2(v0[0], v0[1]), pk2(v0[2], v0[3]), pk2(v0[4], v0[5]), pk2(v0[6], v0[7])};
        *(LAS u32x4*)(d + 256) = (u32x4){pk2(v1[0], v1[1]), pk2(v1[2], v1[3]), pk2(v1[4], v1[5]), pk2(v1[6], v1[7])};
      }
    }
    bar();
    TS(13);
    {
      f32x4 acc[4] = {Z4, Z4, Z4, Z4};
#pragma unroll
      for (int ks = 0; ks < 4; ++ks) {
        u32x4 vf = *(const LAS u32x4*)(sm + L_HB + (n & 3) * 256 + ks * 64 + g * 16);
#pragma unroll
        for (int i = 0; i < 4; ++i) acc[i] = mfma(Fo[4 * ks + i], vf, acc[i]);
      }
      u32x4 ov;
#pragma unroll
      for (int r = 0; r < 4; ++r) {
        const float lo = (n & 4) ? acc[1][r] : acc[0][r], hi = (n & 4) ? acc[3][r] : acc[2][r];
        const float v = (n & 8) ? hi : lo;
        ov[r] = tg8(v + dpp<0x4E>(v), t8);
      }
      if (!(n & 2)) wst16(R, OFF_OUTP + (u32)((n & 1) * 16 + 2 * w + (n >> 3)) * 16384 + chunk * 128 + ((n >> 2) & 1) * 64 + g * 16, ov);
    }
    TS(10);
  } else if (chunk < 32) {
    const int b = chunk >> 4, m = (chunk & 15) * 32 + (l & 31);
    const u32 oo = OFF_OUTP + (u32)chunk * 16384 + w * 2048 + l * 16;
    const u32 go = OFF_MODG + (u32)xcd * 16384 + b * 2048 + m * 4;
    u32 ga = wld4(R, go), gb = wld4(R, go + 12288);
    const float xx = bflo(gld2(x + b * 512 + m));
    for (int it = 0; it < SPIN_MAX; ++it) {
      if (!__any((((ga ^ E) | (gb ^ E)) & 0xFFFFu) != 0u)) break;
      const u32 o = opq(go);
      ga = wld4(R, o); gb = wld4(R, o + 12288);
    }
    u32x4 v0 = wld16(R, oo), v1 = wld16(R, oo + 1024);
    for (int it = 0; it < SPIN_MAX; ++it) {
      const bool n0 = bad8(v0, E), n1 = bad8(v1, E);
      if (!__any(n0 | n1)) break;
      __builtin_amdgcn_s_sleep(1);
      const u32 o = opq(oo);
      if (n0) v0 = wld16(R, o);
      if (n1) v1 = wld16(R, o + 1024);
    }
    TS(11);
    f32x4 s4;
#pragma unroll
    for (int i = 0; i < 4; ++i) { float t = un8(v0[i]) + un8(v1[i]); t += dpp<0x128>(t); s4[i] = x32_sum(x16_sum(t)); }
    if (l < 8) *(LAS f32x4*)(sm + L_RED + w * 128 + l * 16) = s4;
    bar();
    const LAS float* r = (const LAS float*)(sm + L_RED + (l & 31) * 4);
    const float at = rb(((r[0] + r[32]) + (r[64] + r[96])) + ((r[128] + r[160]) + (r[192] + r[224])));
    const float x1 = rb(xx + rb(bfhi(ga) * at));
    if (l < 32) wst4(R, OFF_X1 + (u32)w * 4096 + b * 2048 + m * 4, tg16(x1, t16));
    if (chunk == 0 && tid == 0) wst4(R, 0, E);
    TS(12);
    const u32 fo = OFF_FIN + (u32)chunk * 8192 + w * 896 + (l < 56 ? l : l - 56) * 16;
    u32x4 f = wld16(R, fo);
    for (int it = 0; it < SPIN_MAX; ++it) {
      if (!__any(bad8(f, E))) break;
      __builtin_amdgcn_s_sleep(1);
      f = wld16(R, opq(fo));
    }
    TS(13);
#pragma unroll
    for (int i = 0; i < 4; ++i) { float t = l < 56 ? un8(f[i]) : 0.f; t += dpp<0x128>(t); s4[i] = x32_sum(x16_sum(t)); }
    if (l < 8) *(LAS f32x4*)(sm + L_RED + 1024 + w * 128 + l * 16) = s4;
    bar();
    if (w == 0) {
      const float y = rb(((r[256] + r[288]) + (r[320] + r[352])) + ((r[384] + r[416]) + (r[448] + r[480])));
      const float o = x1 + rb(bfhi(gb) * y);
      if (l < 32) *(GAS u16*)(out + b * 512 + m) = (u16)(pk2(o, o) & 0xFFFFu);
    }
    TS(14);
  } else if (chunk < 64) {
    const u32 pm = mod_cols(chunk - 16);
    const u32 o = OFF_MODG + (u32)w * 16384 + ((chunk - 32) >> 3) * 4096 + ((chunk & 7) * 64 + l) * 4;
    wst4(R, o, (pm << 16) | t16); wst4(R, o + 2048, (pm & 0xFFFF0000u) | t16);
    TS(10);
  } else if (fi >= 0) {
    u32x4 W[8], FG[8];
    {
      const u16* p = (colblk < 4 ? wg : wu) + (size_t)(64 * w + 32 * kg + 8 * g) * 1792 + fi * 32 + 8 * (colblk & 3);
#pragma unroll
      for (int i = 0; i < 8; ++i) W[i] = gld16(p + i * 1792);
    }
    u32x2 Wd[8];
    {
      const u16* p = wd + (size_t)(fi * 32 + 8 * g) * 512 + 64 * w + 4 * n;
#pragma unroll
      for (int i = 0; i < 8; ++i) Wd[i] = gl8<NT_W1>(p + i * 512);
    }
    sched_fence();
    u32x4 Fd[4];
    tr8x8(W, FG);
    tr8x4(Wd, Fd);
#pragma unroll
    for (int i = 0; i < 8; ++i) pin(FG[i]);
#pragma unroll
    for (int i = 0; i < 4; ++i) pin(Fd[i]);
    TS(10);
    if (w < 2) {
      const u32 so = OFF_MODG + (u32)xcd * 16384 + 4096 + (u32)w * 2048 + l * 16;
      u32x4 sb0 = wld16(R, so), sb1 = wld16(R, so + 1024), cb0 = wld16(R, so + 4096), cb1 = wld16(R, so + 5120);
      for (int it = 0; it < SPIN_MAX; ++it) {
        if (!__any(bad16(sb0, E) | bad16(sb1, E) | bad16(cb0, E) | bad16(cb1, E))) break;
        const u32 o = opq(so);
        sb0 = wld16(R, o); sb1 = wld16(R, o + 1024); cb0 = wld16(R, o + 4096); cb1 = wld16(R, o + 5120);
      }
      float c1[8], sh[8];
#pragma unroll
      for (int i = 0; i < 4; ++i) {
        c1[i] = rb(1.f + bfhi(cb0[i])); c1[4 + i] = rb(1.f + bfhi(cb1[i]));
        sh[i] = bfhi(sb0[i]); sh[4 + i] = bfhi(sb1[i]);
      }
#pragma unroll
      for (int i = 0; i < 8; ++i) { pin(c1[i]); pin(sh[i]); }
      TS(11);
      const u32 xo = OFF_X1 + (u32)xcd * 4096 + (u32)w * 2048 + l * 16;
      u32x4 a0 = wld16(R, xo), a1 = wld16(R, xo + 1024);
      for (int it = 0; it < SPIN_MAX; ++it) {
        if (!__any(bad16(a0, E) | bad16(a1, E))) break;
        const u32 o = opq(xo);
        a0 = wld16(R, o); a1 = wld16(R, o + 1024);
      }
      TS(12);
      float xv[8];
#pragma unroll
      for (int i = 0; i < 4; ++i) { xv[i] = bfhi(a0[i]); xv[4 + i] = bfhi(a1[i]); }
      const float m = wave_sum(((xv[0] + xv[1]) + (xv[2] + xv[3])) + ((xv[4] + xv[5]) + (xv[6] + xv[7]))) * (1.f / 512.f);
      float qq = 0.f;
#pragma unroll
      for (int i = 0; i < 8; ++i) { float d = xv[i] - m; qq += d * d; }
      const float var = wave_sum(qq) * (1.f / 512.f);
      const float mean = rb(m), sd = rcp(rb(__builtin_amdgcn_sqrtf(rb(rb(var) + 1e-5f))));
      float hv[8];
#pragma unroll
      for (int i = 0; i < 8; ++i) hv[i] = rb(rb(rb(xv[i] - mean) * sd) * c1[i]) + sh[i];
      *(LAS u32x2*)(sm + L_HB + w * 1024 + l * 8) = (u32x2){pk2(hv[0], hv[1]), pk2(hv[2], hv[3])};
      *(LAS u32x2*)(sm + L_HB + w * 1024 + 512 + l * 8) = (u32x2){pk2(hv[4], hv[5]), pk2(hv[6], hv[7])};
    }
    bar();
    {
      u32x4 af = *(const LAS u32x4*)(sm + L_HB + (n & 1) * 1024 + (64 * w + 32 * ((n >> 1) & 1) + 8 * g) * 2);
      float v0[8], v1[8];
#pragma unroll
      for (int i = 0; i < 8; ++i) {
        f32x4 a = mfma(af, FG[i], Z4);
        float a0 = kg ? a[2] : a[0], a1 = kg ? a[3] : a[1];
        v0[i] = a0 + dpp<0x128>(a0);
        v1[i] = a1 + dpp<0x128>(a1);
      }
      if (l < 8) {
        LAS f32x4* r = (LAS f32x4*)(sm + L_RED + 4096 + w * 512 + l * 32);
        r[0] = (f32x4){v0[0], v0[1], v0[2], v0[3]}; r[1] = (f32x4){v0[4], v0[5], v0[6], v0[7]};
        r[16] = (f32x4){v1[0], v1[1], v1[2], v1[3]}; r[17] = (f32x4){v1[4], v1[5], v1[6], v1[7]};
      }
    }
    bar();
    {
      const LAS float* r = (const LAS float*)(sm + L_RED + 4096 + (l >> 5) * 256 + (l & 31) * 4);
      float gs = ((r[0] + r[128]) + (r[256] + r[384])) + ((r[512] + r[640]) + (r[768] + r[896]));
      float us = ((r[32] + r[160]) + (r[288] + r[416])) + ((r[544] + r[672]) + (r[800] + r[928]));
      float hv = rb(gelu_f(rb(gs))) * rb(us);
      *(LAS u16*)(sm + L_P + w * 128 + l * 2) = (u16)(pk2(hv, hv) & 0xFFFFu);
      lds_wait();
      u32x4 ha = *(const LAS u32x4*)(sm + L_P + w * 128 + (n & 1) * 64 + g * 16);
      u32x4 o;
#pragma unroll
      for (int i = 0; i < 4; ++i) {
        f32x4 a = mfma(ha, Fd[i], Z4);
        o[i] = tg8((g & 1) ? a[1] : a[0], t8);
      }
      if (g < 2) wst16(R, OFF_FIN + (u32)(g * 16 + 2 * w + (n >> 3)) * 8192 + fi * 128 + (n & 7) * 16, o);
    }
    TS(13);
  }
}

extern "C" void kernel_launch(void* const* d_in, const int* in_sizes, int n_in, void* d_out, int out_size, void* d_ws,
                              size_t ws_size, hipStream_t stream) {
  (void)in_sizes; (void)n_in; (void)out_size; (void)ws_size;
  (void)hipFuncSetAttribute((const void*)fused_kernel, hipFuncAttributeMaxDynamicSharedMemorySize, (int)LDS_BYTES);
  hipLaunchKernelGGL(fused_kernel, dim3(256), dim3(512), LDS_BYTES, stream, (const u16*)d_in[0], (const u16*)d_in[1],
                     (const u16*)d_in[2], (const u16*)d_in[3], (const u16*)d_in[4], (const u16*)d_in[5], (const u16*)d_in[7],
                     (const u16*)d_in[8], (const u16*)d_in[9], (const u16*)d_in[10], (const u16*)d_in[11], (u16*)d_out, (char*)d_ws);
}
```

```cpp
#include <hip/hip_runtime.h>
#include <hip/hip_bf16.h>
#include <stdint.h>
#include <type_traits>

typedef unsigned short u16;
typedef unsigned int u32;
typedef __attribute__((ext_vector_type(4))) u32 u32x4;
typedef __attribute__((ext_vector_type(2))) u32 u32x2;
typedef __attribute__((ext_vector_type(4))) float f32x4;
typedef __attribute__((ext_vector_type(2))) float f32x2;
typedef __attribute__((ext_vector_type(8))) __bf16 bf16x8;
typedef __attribute__((ext_vector_type(2))) __bf16 bf16x2;
typedef __attribute__((ext_vector_type(4))) short s16x4;
#define LAS __attribute__((address_space(3)))
#define GAS __attribute__((address_space(1)))
#define DI __device__ __forceinline__

constexpr u32 OFF_HINT = 4096;
constexpr u32 OFF_HM = OFF_HINT + 16384;
constexpr u32 OFF_MODG = OFF_HM + 65536;
constexpr u32 OFF_X1 = OFF_MODG + 131072;
constexpr u32 OFF_QA = OFF_X1 + 32768;
constexpr u32 OFF_QK = OFF_QA + 131072;
constexpr u32 OFF_OP = OFF_QK + 65536;
constexpr u32 OFF_ML = OFF_OP + 4194304;
constexpr u32 OFF_OUTP = OFF_ML + 524288;
constexpr u32 OFF_FIN = OFF_OUTP + 524288;
constexpr u32 WS_BYTES = OFF_FIN + 262144;
constexpr u32 OFF_DBG = 8u << 20;
static_assert(WS_BYTES <= OFF_DBG, "ws");
constexpr int SPIN_MAX = 1 << 16;

constexpr u32 L_C = 0;
constexpr u32 L_Q = 0;
constexpr u32 L_P = 32768;
constexpr u32 L_MX = L_P + 8704;
constexpr u32 L_LS = L_MX + 1024;
constexpr u32 L_RED = 131072;
constexpr u32 L_HB = L_RED + 8192;
constexpr u32 L_QB = L_HB + 2048;
constexpr u32 L_DUM = L_QB + 512;
constexpr u32 LDS_BYTES = L_DUM + 256;
static_assert(LDS_BYTES <= 163840, "lds");

DI float bflo(u32 v) { return __builtin_bit_cast(float, v << 16); }
DI float bfhi(u32 v) { return __builtin_bit_cast(float, v & 0xFFFF0000u); }
DI u32 pk2(float a, float b) {
  f32x2 f = {a, b};
  return __builtin_bit_cast(u32, __builtin_convertvector(f, bf16x2));
}
DI float rb(float a) { return bflo(pk2(a, a)); }
DI f32x4 mfma(u32x4 a, u32x4 b, f32x4 c) {
  return __builtin_amdgcn_mfma_f32_16x16x32_bf16(__builtin_bit_cast(bf16x8, a), __builtin_bit_cast(bf16x8, b), c, 0, 0, 0);
}
template <int CTRL> DI float dpp(float x) {
  return __builtin_bit_cast(float, __builtin_amdgcn_update_dpp(0, __builtin_bit_cast(int, x), CTRL, 0xF, 0xF, true));
}
DI float x16_sum(float x) { auto s = __builtin_amdgcn_permlane16_swap(__float_as_uint(x), __float_as_uint(x), false, false); return __uint_as_float(s[0]) + __uint_as_float(s[1]); }
DI float x32_sum(float x) { auto s = __builtin_amdgcn_permlane32_swap(__float_as_uint(x), __float_as_uint(x), false, false); return __uint_as_float(s[0]) + __uint_as_float(s[1]); }
DI float x16_max(float x) { auto s = __builtin_amdgcn_permlane16_swap(__float_as_uint(x), __float_as_uint(x), false, false); return fmaxf(__uint_as_float(s[0]), __uint_as_float(s[1])); }
DI float x32_max(float x) { auto s = __builtin_amdgcn_permlane32_swap(__float_as_uint(x), __float_as_uint(x), false, false); return fmaxf(__uint_as_float(s[0]), __uint_as_float(s[1])); }
DI float wave_sum(float x) {
  x += dpp<0xB1>(x); x += dpp<0x4E>(x); x += dpp<0x141>(x); x += dpp<0x140>(x);
  return x32_sum(x16_sum(x));
}
DI float wave_max(float x) {
  x = fmaxf(x, dpp<0xB1>(x)); x = fmaxf(x, dpp<0x4E>(x)); x = fmaxf(x, dpp<0x141>(x)); x = fmaxf(x, dpp<0x140>(x));
  return x32_max(x16_max(x));
}
DI float silu_f(float x) { return x * __builtin_amdgcn_rcpf(1.f + __expf(-x)); }
DI float gelu_f(float g) {
  float z = 0.7978845608028654f * (g + 0.044715f * g * g * g);
  float e = __expf(2.f * z);
  float t = 1.f - 2.f * __builtin_amdgcn_rcpf(e + 1.f);
  return 0.5f * g * (1.f + t);
}
DI u32x4 gld16(const void* p) { return *(const GAS u32x4*)p; }
DI u32x2 gld8(const void* p) { return *(const GAS u32x2*)p; }
DI u32 gld2(const void* p) { return *(const GAS u16*)p; }
typedef __amdgpu_buffer_rsrc_t rsrc_t;
DI u32x4 wld16(rsrc_t r, u32 off) { return __builtin_amdgcn_raw_buffer_load_b128(r, off, 0, 16); }
DI u32x2 wld8(rsrc_t r, u32 off) { return __builtin_amdgcn_raw_buffer_load_b64(r, off, 0, 16); }
DI u32 wld4(rsrc_t r, u32 off) { return __builtin_amdgcn_raw_buffer_load_b32(r, off, 0, 16); }
DI void wst16(rsrc_t r, u32 off, u32x4 v) { __builtin_amdgcn_raw_buffer_store_b128(v, r, off, 0, 16); }
DI void wst8(rsrc_t r, u32 off, u32x2 v) { __builtin_amdgcn_raw_buffer_store_b64(v, r, off, 0, 16); }
DI void wst4(rsrc_t r, u32 off, u32 v) { __builtin_amdgcn_raw_buffer_store_b32(v, r, off, 0, 16); }
DI u32 opq(u32 v) { asm volatile("" : "+v"(v)); return v; }
template <class T> DI void pin(T& v) { asm volatile("" : "+v"(v)); }
DI float rcp(float x) { return __builtin_amdgcn_rcpf(x); }
DI void fence_c() { asm volatile("" ::: "memory"); }
DI void sched_fence() { __builtin_amdgcn_sched_barrier(0); }
DI void lds_wait() { asm volatile("s_waitcnt lgkmcnt(0)" ::: "memory"); }
DI void bar() { asm volatile("s_waitcnt lgkmcnt(0)\n\ts_barrier" ::: "memory"); }
DI void tr8x8(const u32x4 (&W)[8], u32x4 (&F)[8]) {
#pragma unroll
  for (int i = 0; i < 8; ++i)
#pragma unroll
    for (int p = 0; p < 4; ++p)
      F[i][p] = __builtin_amdgcn_perm(W[2 * p + 1][i >> 1], W[2 * p][i >> 1], (i & 1) ? 0x07060302u : 0x05040100u);
}
DI void tr8x4(const u32x2* W, u32x4* F) {
#pragma unroll
  for (int i = 0; i < 4; ++i)
#pragma unroll
    for (int p = 0; p < 4; ++p)
      F[i][p] = __builtin_amdgcn_perm(W[2 * p + 1][i >> 1], W[2 * p][i >> 1], (i & 1) ? 0x07060302u : 0x05040100u);
}
DI void ln_stats(u32x4 v, float& mean, float& sd) {
  float s = 0.f;
#pragma unroll
  for (int i = 0; i < 4; ++i) s += bflo(v[i]) + bfhi(v[i]);
  float m = wave_sum(s) * (1.f / 512.f);
  float q = 0.f;
#pragma unroll
  for (int i = 0; i < 4; ++i) { float a = bflo(v[i]) - m, b = bfhi(v[i]) - m; q += a * a + b * b; }
  float var = wave_sum(q) * (1.f / 512.f);
  mean = rb(m);
  sd = __builtin_amdgcn_rcpf(rb(__builtin_amdgcn_sqrtf(rb(rb(var) + 1e-5f))));
}
DI float ln_mod(float x, float mean, float sd, float sc, float sh) {
  float ln = rb(rb(x - mean) * sd);
  return rb(rb(ln * rb(1.f + sc)) + sh);
}

#define PF_TLB 1
#define PF_CODE 1
#define PF_NW 6
#define NT_WQ 1
#define NT_CB 1
#define NT_CA 0
#define NT_W1 1
#define HINT_SLEEP 40
#define B_DELAY 60
#define OP_SLEEP 0
#define B_GATE 0
#define A_EARLY 0
#define ROT 0
#define OP_TH 64
#define OP_PS 4
#define WO_EARLY 1
#define NT_WO 1
template <int NT> DI u32x4 gl16(const void* p) {
  if (NT) return __builtin_nontemporal_load((const GAS u32x4*)p);
  return *(const GAS u32x4*)p;
}
template <int NT> DI u32x2 gl8(const void* p) {
  if (NT) return __builtin_nontemporal_load((const GAS u32x2*)p);
  return *(const GAS u32x2*)p;
}
template <int NT> DI void dma16(const void* gp, u32 lds) {
  u32 keep;
  if (NT) asm volatile("s_mov_b32 %0, m0\n\ts_mov_b32 m0, %2\n\ts_nop 0\n\tglobal_load_lds_dwordx4 %1, off nt\n\ts_mov_b32 m0, %0" : "=&s"(keep) : "v"(gp), "s"(lds) : "memory");
  else asm volatile("s_mov_b32 %0, m0\n\ts_mov_b32 m0, %2\n\ts_nop 0\n\tglobal_load_lds_dwordx4 %1, off\n\ts_mov_b32 m0, %0" : "=&s"(keep) : "v"(gp), "s"(lds) : "memory");
}
template <int SC> DI void touch4(const void* gp, u32 lds) {
  u32 keep;
  if (SC) asm volatile("s_mov_b32 %0, m0\n\ts_mov_b32 m0, %2\n\ts_nop 0\n\tglobal_load_lds_dword %1, off sc1\n\ts_mov_b32 m0, %0" : "=&s"(keep) : "v"(gp), "s"(lds) : "memory");
  else asm volatile("s_mov_b32 %0, m0\n\ts_mov_b32 m0, %2\n\ts_nop 0\n\tglobal_load_lds_dword %1, off\n\ts_mov_b32 m0, %0" : "=&s"(keep) : "v"(gp), "s"(lds) : "memory");
}
template <int CTRL> DI u32 dppu(u32 x) { return (u32)__builtin_amdgcn_update_dpp(0, (int)x, CTRL, 0xF, 0xF, true); }
DI u32 sum8l(u32 x) { x += dppu<0xB1>(x); x += dppu<0x4E>(x); x += dppu<0x141>(x); return x; }
DI u32 sum16l(u32 x) { x = sum8l(x); x += dppu<0x140>(x); return x; }
DI bool bad16x2(u32x2 v, u32 t) { return (((v[0] ^ t) | (v[1] ^ t)) & 0xFFFFu) != 0u; }
DI void vm_wait0() { asm volatile("s_waitcnt vmcnt(0)" ::: "memory"); }
DI u32 sum4(u32x4 v) { return (v[0] + v[1]) + (v[2] + v[3]); }
DI bool bad16(u32x4 v, u32 t) { return (((v[0] ^ t) | (v[1] ^ t) | (v[2] ^ t) | (v[3] ^ t)) & 0xFFFFu) != 0u; }
DI bool bad8(u32x4 v, u32 t) { return (((v[0] ^ t) | (v[1] ^ t) | (v[2] ^ t) | (v[3] ^ t)) & 0xFFu) != 0u; }
DI u32 tg16(float bfval, u32 t16) { return (__float_as_uint(bfval) & 0xFFFF0000u) | t16; }
DI u32 tg8(float v, u32 t8) { return (__float_as_uint(v) & 0xFFFFFF00u) | t8; }
DI float un8(u32 v) { return __uint_as_float(v & 0xFFFFFF00u); }

#define TS(i) do {} while (0)

__global__ void __launch_bounds__(512, 2)
fused_kernel(const u16* __restrict__ x, const u16* __restrict__ ckv, const u16* __restrict__ wq, const u16* __restrict__ wuk,
             const u16* __restrict__ wuv, const u16* __restrict__ wo, const u16* __restrict__ wg, const u16* __restrict__ wu,
             const u16* __restrict__ wd, const u16* __restrict__ cnd, const u16* __restrict__ wmod, u16* __restrict__ out,
             char* ws) {
  extern __shared__ __attribute__((aligned(16))) char smem_raw[];
  LAS char* sm = (LAS char*)smem_raw;
  const u32 lds0 = (u32)(size_t)sm;
  const int tid = threadIdx.x, l = tid & 63;
  const int w = __builtin_amdgcn_readfirstlane(tid >> 6);
  const int n = l & 15, g = l >> 4;
  const int bid = blockIdx.x, xcd = bid & 7, j = bid >> 3;
  const int cls = (j >= 2 && j < 18) ? 0 : 1, pairidx = j < 2 ? j : (j < 18 ? j - 2 : j - 16);
  const int chunk = xcd + 8 * pairidx;
  const int h2 = 4 * xcd + (pairidx >> 2), cq = pairidx & 3;
  const int fi = chunk - 72;
  const rsrc_t R = __builtin_amdgcn_make_buffer_rsrc(ws, 0, WS_BYTES, 0x00020000);
  const f32x4 Z4 = {0.f, 0.f, 0.f, 0.f};
  TS(0);
  const u32 Ev = *(const GAS u32*)ws + 1u;
  const int colblk = n & 7, kg = n >> 3;
  u32x4 Wv[8];
  const int rot = (pairidx + 2 * w) & 15;
  auto load_rows = [&](const u16* src, int r0, auto nr, auto nt, int ib = 0) {
#pragma unroll
    for (int i = 0; i < decltype(nr)::value; ++i) {
      if (ROT && decltype(nr)::value == 16) {
        const int ii = (i + rot) & 15;
        const u32 swz = ((ii & 3) << 2) | (ii >> 2);
        dma16<decltype(nt)::value>(src + (size_t)(r0 + ii) * 512 + (l ^ swz) * 8, lds0 + L_C + (r0 + ii) * 1024);
      } else {
        const u32 swz = (((i + ib) & 3) << 2) | (((i + ib) >> 2) & 3);
        dma16<decltype(nt)::value>(src + (size_t)(r0 + i) * 512 + (l ^ swz) * 8, lds0 + L_C + (r0 + i) * 1024);
      }
    }
  };
  auto read_tr = [&](u32x4 (&f)[16]) {
    const int q = n >> 2, pp = n & 3;
#pragma unroll
    for (int ks = 0; ks < 4; ++ks)
#pragma unroll
      for (int t = 0; t < 4; ++t)
#pragma unroll
        for (int tt = 0; tt < 2; ++tt) {
          const int row = 32 * ks + 8 * g + 4 * tt + q;
          const u32 swz = (q << 2) | ((2 * g + tt) & 3);
          const u32 ad = L_C + row * 1024 + (((8 * w + 2 * t + (pp >> 1)) ^ swz) * 16) + 8 * (pp & 1);
          u32x2 v = __builtin_bit_cast(u32x2, __builtin_amdgcn_ds_read_tr16_b64_v4i16((LAS s16x4*)(sm + ad)));
          f[4 * ks + t][2 * tt] = v[0]; f[4 * ks + t][2 * tt + 1] = v[1];
        }
  };
  auto mod_cols = [&](int cb) -> u32 {
    u32x4 cfr = gld16(cnd + (n & 1) * 512 + 64 * w + 32 * ((n >> 1) & 1) + 8 * g);
    u32x4 W[8];
    const u16* p = wmod + (size_t)(64 * w + 32 * kg + 8 * g) * 3072 + cb * 64 + 8 * colblk;
#pragma unroll
    for (int i = 0; i < 8; ++i) W[i] = gl16<NT_W1>(p + i * 3072);
    sched_fence();
    u32x4 af;
#pragma unroll
    for (int i = 0; i < 4; ++i) af[i] = pk2(silu_f(bflo(cfr[i])), silu_f(bfhi(cfr[i])));
    pin(af);
    u32x4 F[8];
    tr8x8(W, F);
    float v0[8], v1[8];
#pragma unroll
    for (int i = 0; i < 8; ++i) {
      f32x4 a = mfma(af, F[i], Z4);
      float a0 = kg ? a[2] : a[0], a1 = kg ? a[3] : a[1];
      v0[i] = a0 + dpp<0x128>(a0);
      v1[i] = a1 + dpp<0x128>(a1);
    }
    if (l < 8) {
      LAS f32x4* r = (LAS f32x4*)(sm + L_RED + w * 512 + l * 32);
      r[0] = (f32x4){v0[0], v0[1], v0[2], v0[3]}; r[1] = (f32x4){v0[4], v0[5], v0[6], v0[7]};
      r[16] = (f32x4){v1[0], v1[1], v1[2], v1[3]}; r[17] = (f32x4){v1[4], v1[5], v1[6], v1[7]};
    }
    bar();
    const LAS float* r = (const LAS float*)(sm + L_RED + l * 4);
    float m0 = ((r[0] + r[128]) + (r[256] + r[384])) + ((r[512] + r[640]) + (r[768] + r[896]));
    float m1 = ((r[64] + r[192]) + (r[320] + r[448])) + ((r[576] + r[704]) + (r[832] + r[960]));
    return pk2(m0, m1);
  };
  typedef std::integral_constant<int, 16> I16;
  typedef std::integral_constant<int, 32> I32;
  const u16* crow = ckv + (size_t)chunk * 128 * 512;
  const u16* worow = wo + (size_t)h2 * 128 * 512;
  u32x4 cf[16], ca[16];
  auto read_frags = [&]() {
    const u32 swz = ((n & 3) << 2) | (n >> 2);
#pragma unroll
    for (int ks = 0; ks < 16; ++ks) cf[ks] = *(const LAS u32x4*)(sm + L_C + (16 * w + n) * 1024 + (((4 * ks + g) ^ swz) * 16));
    read_tr(ca);
#pragma unroll
    for (int ks = 0; ks < 16; ++ks) { pin(cf[ks]); pin(ca[ks]); }
  };
  auto load_s4 = [&]() {
    if (w < 4) {
      const u16* p = wuv + (size_t)(cq * 128 + (w >> 1) * 64 + 32 * kg + 4 * g) * 4096 + h2 * 128 + 64 * (w & 1) + 8 * colblk;
#pragma unroll
      for (int i = 0; i < 8; ++i) Wv[i] = gl16<NT_W1>(p + (size_t)(16 * (i >> 2) + (i & 3)) * 4096);
    }
    load_rows(worow, 16 * w, I16(), std::integral_constant<int, NT_WO>());
  };
  auto hint_wait = [&](u32 e) {
    if (w == 0) {
      const u32 ho = OFF_HINT + (u32)xcd * 2048 + n * 128;
      for (int it = 0; it < SPIN_MAX; ++it) {
        if (!__any(wld4(R, opq(ho)) != e)) break;
        __builtin_amdgcn_s_sleep(2);
      }
      if (HINT_SLEEP) __builtin_amdgcn_s_sleep(HINT_SLEEP);
    }
    bar();
  };
  auto warm_pages = [&]() {
    if (PF_TLB && w == 7) {
      const char* pa = ws + min((u32)l * 131072u, WS_BYTES - 128u);
      if (l == 63) pa = (const char*)out;
      if (l == 62) pa = (const char*)(wuv + (size_t)cq * 128 * 4096);
      if (l == 61) pa = (const char*)(wuv + (size_t)(cq * 128 + 127) * 4096);
      if (l == 60) pa = (const char*)worow;
      if (l == 59) pa = (const char*)wg;
      if (l == 58) pa = (const char*)(wg + 512 * 1792 - 64);
      if (l == 57) pa = (const char*)wu;
      if (l == 56) pa = (const char*)(wu + 512 * 1792 - 64);
      if (l == 55) pa = (const char*)wd;
      if (l == 54) pa = (const char*)(wd + 512 * 1792 - 64);
      if (l == 53) pa = (const char*)x;
      touch4<1>(pa, lds0 + L_DUM);
    }
  };
  auto warm_code = [&]() {
    if (PF_CODE) {
      unsigned long long pc; u32 len;
      asm volatile("s_getpc_b64 %0\n.Lwc_a:\n\ts_mov_b32 %1, .Lwc_b-.Lwc_a" : "=s"(pc), "=s"(len));
      touch4<0>((const char*)pc + min((u32)tid * 128u, len - 4u), lds0 + L_DUM);
    }
  };
  const u32 rb_ = (u32)(xcd & 1) * 2 + (chunk >> 6);
  u32 E;

  if (cls == 0) {
    u32x4 xv0 = gld16(x + 8 * l), xv1 = gld16(x + 512 + 8 * l);
    const u32 xm0 = *(const GAS u32*)(x + 2 * (tid & 255)), xm1 = *(const GAS u32*)(x + 512 + 2 * (tid & 255));
    u32x4 W[16];
    {
      const u16* p = wq + (size_t)(64 * w + 8 * g) * 4096 + h2 * 128 + 8 * n;
#pragma unroll
      for (int i = 0; i < 16; ++i) W[i] = gl16<NT_WQ>(p + (size_t)(32 * (i >> 3) + (i & 7)) * 4096);
    }
#pragma unroll
    for (int i = 0; i < 4; ++i)
      dma16<NT_W1>(wuk + (size_t)(128 * cq + 16 * w + 4 * i + g) * 4096 + h2 * 128 + (n ^ (4 * i + g)) * 8, lds0 + L_C + w * 16384 + 12288 + i * 1024);
    warm_pages();
    sched_fence();
    E = __builtin_amdgcn_readfirstlane(Ev);
    float mean0, sd0, mean1, sd1;
    ln_stats(xv0, mean0, sd0);
    ln_stats(xv1, mean1, sd1);
    float ln00 = rb(rb(bflo(xm0) - mean0) * sd0), ln01 = rb(rb(bflo(xm1) - mean1) * sd1);
    float ln10 = rb(rb(bfhi(xm0) - mean0) * sd0), ln11 = rb(rb(bfhi(xm1) - mean1) * sd1);
    u32x4 F[16];
    tr8x8(*(const u32x4(*)[8])(W), *(u32x4(*)[8])(F));
    tr8x8(*(const u32x4(*)[8])(W + 8), *(u32x4(*)[8])(F + 8));
#pragma unroll
    for (int i = 0; i < 16; ++i) pin(F[i]);
    pin(ln00); pin(ln01); pin(ln10); pin(ln11);
    vm_wait0();
    u32x4 uk[4];
#pragma unroll
    for (int ks = 0; ks < 4; ++ks) uk[ks] = *(const LAS u32x4*)(sm + L_C + w * 16384 + 12288 + n * 256 + (((4 * ks + g) ^ n) * 16));
#pragma unroll
    for (int i = 0; i < 4; ++i) pin(uk[i]);
    TS(1);
    if (w < 4) {
      const u32 ho = OFF_HM + (u32)xcd * 8192 + tid * 16;
      u32x4 hs = wld16(R, ho), hc = wld16(R, ho + 4096);
      for (int it = 0; it < SPIN_MAX; ++it) {
        if (!__any(bad16(hs, E) | bad16(hc, E))) break;
        const u32 o = opq(ho);
        hs = wld16(R, o); hc = wld16(R, o + 4096);
      }
      const float h00 = rb(ln00 * rb(1.f + bfhi(hc[0]))) + bfhi(hs[0]), h01 = rb(ln01 * rb(1.f + bfhi(hc[1]))) + bfhi(hs[1]);
      const float h10 = rb(ln10 * rb(1.f + bfhi(hc[2]))) + bfhi(hs[2]), h11 = rb(ln11 * rb(1.f + bfhi(hc[3]))) + bfhi(hs[3]);
      *(LAS u32*)(sm + L_HB + tid * 4) = pk2(h00, h10);
      *(LAS u32*)(sm + L_HB + 1024 + tid * 4) = pk2(h01, h11);
    }
    TS(2);
    bar();
    {
      u32x4 a0 = *(const LAS u32x4*)(sm + L_HB + (n & 1) * 1024 + w * 128 + g * 16);
      u32x4 a1 = *(const LAS u32x4*)(sm + L_HB + (n & 1) * 1024 + w * 128 + 64 + g * 16);
      f32x4 acc[8];
#pragma unroll
      for (int i = 0; i < 8; ++i) acc[i] = mfma(a0, F[i], Z4);
#pragma unroll
      for (int i = 0; i < 8; ++i) acc[i] = mfma(a1, F[8 + i], acc[i]);
      if (g == 0) {
        LAS f32x4* r = (LAS f32x4*)(sm + L_RED + w * 1024 + n * 32);
        r[0] = (f32x4){acc[0][0], acc[1][0], acc[2][0], acc[3][0]}; r[1] = (f32x4){acc[4][0], acc[5][0], acc[6][0], acc[7][0]};
        r[32] = (f32x4){acc[0][1], acc[1][1], acc[2][1], acc[3][1]}; r[33] = (f32x4){acc[4][1], acc[5][1], acc[6][1], acc[7][1]};
      }
    }
    bar();
    if (w < 4) {
      const LAS float* r = (const LAS float*)(sm + L_RED + tid * 4);
      float s = ((r[0] + r[256]) + (r[512] + r[768])) + ((r[1024] + r[1280]) + (r[1536] + r[1792]));
      *(LAS u16*)(sm + L_QB + tid * 2) = (u16)(pk2(s, s) & 0xFFFFu);
    }
    bar();
    {
      u32x4 qb[4];
#pragma unroll
      for (int ks = 0; ks < 4; ++ks) qb[ks] = *(const LAS u32x4*)(sm + L_QB + (n & 1) * 256 + ks * 64 + g * 16);
      f32x4 d0 = mfma(uk[0], qb[0], Z4), d1 = mfma(uk[1], qb[1], Z4);
      d0 = mfma(uk[2], qb[2], d0); d1 = mfma(uk[3], qb[3], d1);
      d0 += d1;
      if (n < 2) *(LAS u32x2*)(sm + L_HB + n * 256 + w * 32 + g * 8) = (u32x2){pk2(d0[0], d0[1]), pk2(d0[2], d0[3])};
    }
    bar();
    if (w == 0) {
      const u32x4 pay = *(const LAS u32x4*)(sm + L_HB + (g & 1) * 256 + n * 16);
      wst16(R, OFF_QA + (u32)(g * 32 + h2) * 1024 + cq * 256 + n * 16, pay);
      const u32 ck = sum16l(sum4(pay)) + E;
      if (n < 8) wst16(R, OFF_QK + (u32)((g * 32 + h2) * 4 + cq) * 128 + n * 16, (u32x4){ck, ck, ck, ck});
    }
    TS(3);
    load_s4();
  } else {
    if (chunk < 16) {
      const u32 pm = mod_cols(chunk);
      TS(1);
      E = __builtin_amdgcn_readfirstlane(Ev);
      const u32 tt16 = E & 0xFFFFu;
      wst8(R, OFF_HM + (u32)w * 8192 + (chunk >> 3) * 4096 + ((chunk & 7) * 64 + l) * 8, (u32x2){(pm << 16) | tt16, (pm & 0xFFFF0000u) | tt16});
      if (l < 8) wst16(R, OFF_HINT + (u32)w * 2048 + chunk * 128 + l * 16, (u32x4){E, E, E, E});
      TS(2);
      warm_pages();
    } else {
      if (pairidx < PF_NW) warm_code();
      warm_pages();
      if (B_DELAY) __builtin_amdgcn_s_sleep(B_DELAY);
    }
    load_rows(crow, 16 * w, I16(), std::integral_constant<int, NT_CB>());
    E = __builtin_amdgcn_readfirstlane(Ev);
    vm_wait0();
    bar();
    TS(3);
    read_frags();
    TS(4);
    hint_wait(E);
    {
      const u32 qo = OFF_QA + (rb_ * 32 + 4 * w) * 1024 + l * 16, ko = OFF_QK + ((rb_ * 32 + 4 * w + (n & 3)) * 4 + g) * 128;
      u32x4 q[4];
#pragma unroll
      for (int i = 0; i < 4; ++i) q[i] = wld16(R, qo + i * 1024);
      u32 qk = wld4(R, ko);
      for (int it = 0; it < SPIN_MAX; ++it) {
        const u32 c0 = sum16l(sum4(q[0])), c1 = sum16l(sum4(q[1])), c2 = sum16l(sum4(q[2])), c3 = sum16l(sum4(q[3]));
        const u32 cs = (n & 2) ? ((n & 1) ? c3 : c2) : ((n & 1) ? c1 : c0);
        const unsigned long long bad = __ballot(cs + E != qk);
        if (!bad) break;
        const u32 z = opq(0u);
#pragma unroll
        for (int i = 0; i < 4; ++i)
          if (bad & (0x1111111111111111ull << i)) q[i] = wld16(R, qo + z + i * 1024);
        qk = wld4(R, ko + z);
      }
#pragma unroll
      for (int i = 0; i < 4; ++i) *(LAS u32x4*)(sm + L_Q + (4 * w + i) * 1024 + (l ^ ((i << 2) | (w & 3))) * 16) = q[i];
    }
    TS(5);
    sched_fence();
    bar();
    TS(6);
    f32x4 sa[2];
    {
      const u32 qswz = ((n & 3) << 2) | (n >> 2);
      u32x4 qf[2][8];
#pragma unroll
      for (int u = 0; u < 2; ++u)
#pragma unroll
        for (int ks = 0; ks < 8; ++ks) qf[u][ks] = *(const LAS u32x4*)(sm + L_Q + n * 1024 + ((4 * (8 * u + ks) + g) ^ qswz) * 16);
      sched_fence();
#pragma unroll
      for (int hh = 0; hh < 2; ++hh) {
        f32x4 s0 = Z4, s1 = Z4, s2 = Z4, s3 = Z4;
#pragma unroll
        for (int u = 0; u < 2; ++u) {
#pragma unroll
          for (int ks = 0; ks < 8; ks += 4) {
            s0 = mfma(cf[8 * u + ks], qf[u][ks], s0); s1 = mfma(cf[8 * u + ks + 1], qf[u][ks + 1], s1);
            s2 = mfma(cf[8 * u + ks + 2], qf[u][ks + 2], s2); s3 = mfma(cf[8 * u + ks + 3], qf[u][ks + 3], s3);
          }
          if (hh == 0) {
#pragma unroll
            for (int ks = 0; ks < 8; ++ks) qf[u][ks] = *(const LAS u32x4*)(sm + L_Q + (16 + n) * 1024 + ((4 * (8 * u + ks) + g) ^ qswz) * 16);
          }
          sched_fence();
        }
        sa[hh] = (s0 + s1) + (s2 + s3);
      }
    }
    float sc[2][4], mx[2];
#pragma unroll
    for (int hh = 0; hh < 2; ++hh) {
#pragma unroll
      for (int r = 0; r < 4; ++r) sc[hh][r] = rb(rb(sa[hh][r]) * 0.08838834764831845f);
      mx[hh] = x32_max(x16_max(fmaxf(fmaxf(sc[hh][0], sc[hh][1]), fmaxf(sc[hh][2], sc[hh][3]))));
    }
    if (g < 2) *(LAS float*)(sm + L_MX + (16 * g + n) * 32 + w * 4) = g ? mx[1] : mx[0];
    TS(9);
    bar();
    TS(15);
#pragma unroll
    for (int hh = 0; hh < 2; ++hh) {
      f32x4 m0 = *(const LAS f32x4*)(sm + L_MX + (16 * hh + n) * 32), m1 = *(const LAS f32x4*)(sm + L_MX + (16 * hh + n) * 32 + 16);
      mx[hh] = fmaxf(fmaxf(fmaxf(m0[0], m0[1]), fmaxf(m0[2], m0[3])), fmaxf(fmaxf(m1[0], m1[1]), fmaxf(m1[2], m1[3])));
    }
    float ls[2];
#pragma unroll
    for (int hh = 0; hh < 2; ++hh) {
      float p[4];
#pragma unroll
      for (int r = 0; r < 4; ++r) p[r] = __expf(sc[hh][r] - mx[hh]);
      *(LAS u32x2*)(sm + L_P + (16 * hh + n) * 272 + (16 * w + 4 * g) * 2) = (u32x2){pk2(p[0], p[1]), pk2(p[2], p[3])};
      ls[hh] = x32_sum(x16_sum((p[0] + p[1]) + (p[2] + p[3])));
    }
    if (g < 2) *(LAS float*)(sm + L_LS + (16 * g + n) * 32 + w * 4) = g ? ls[1] : ls[0];
    bar();
    TS(7);
#pragma unroll
    for (int hh = 0; hh < 2; ++hh) {
      f32x4 o[4] = {Z4, Z4, Z4, Z4};
#pragma unroll
      for (int ks = 0; ks < 4; ++ks) {
        u32x4 pf = *(const LAS u32x4*)(sm + L_P + (16 * hh + n) * 272 + (32 * ks + 8 * g) * 2);
#pragma unroll
        for (int t = 0; t < 4; ++t) o[t] = mfma(ca[4 * ks + t], pf, o[t]);
      }
      f32x4 l0 = *(const LAS f32x4*)(sm + L_LS + (16 * hh + n) * 32), l1 = *(const LAS f32x4*)(sm + L_LS + (16 * hh + n) * 32 + 16);
      const u32x4 pa = {pk2(o[0][0], o[0][1]), pk2(o[0][2], o[0][3]), pk2(o[1][0], o[1][1]), pk2(o[1][2], o[1][3])};
      const u32x4 pb = {pk2(o[2][0], o[2][1]), pk2(o[2][2], o[2][3]), pk2(o[3][0], o[3][1]), pk2(o[3][2], o[3][3])};
      u32x4 px;
#pragma unroll
      for (int i = 0; i < 4; ++i) px[i] = dppu<0x128>(pb[i]);
      const u32 r0 = OFF_OP + (u32)(((16 * hh + (n & 7)) * 8 + w) * 128 + chunk) * 128 + g * 32;
      wst16(R, r0 + (n & 8) * 2, (n & 8) ? px : pa);
      wst16(R, r0 + 8 * 8 * 128 * 128 + 16 - (n & 8) * 2, (n & 8) ? pa : px);
      u32 rs = sum4(pa) + sum4(pb);
      { auto t = __builtin_amdgcn_permlane16_swap(rs, rs, false, false); rs = t[0] + t[1]; }
      { auto t = __builtin_amdgcn_permlane32_swap(rs, rs, false, false); rs = t[0] + t[1]; }
      const float lt = ((l0[0] + l0[1]) + (l0[2] + l0[3])) + ((l1[0] + l1[1]) + (l1[2] + l1[3]));
      const u32 um = __float_as_uint(mx[hh]), ul = __float_as_uint(lt);
      if (g == 0) wst16(R, OFF_ML + (u32)((w * 128 + chunk) * 32 + 16 * hh + n) * 16, (u32x4){um, ul, rs + E, um + ul + E});
    }
    bar();
    TS(8);
  }
  const u32 t8 = E & 0xFFu, t16 = E & 0xFFFFu;
  if (cls == 0) {
    const u32 base = (u32)(h2 * 8 + 2 * cq) * 128;
    const u32 s0i = 16 * w + (l >> 3);
    const u32 oo = OFF_OP + (base + s0i) * 128 + (l & 7) * 16;
    const u32 kq = OFF_ML + ((2 * cq * 128 + s0i) * 32 + h2) * 16;
    u32x4 o[4], k[4];
    vm_wait0();
    bar();
    TS(4);
    u32x4 Fv[8], Fo[16];
    tr8x8(Wv, Fv);
    read_tr(Fo);
#pragma unroll
    for (int i = 0; i < 16; ++i) pin(Fo[i]);
#pragma unroll
    for (int i = 0; i < 8; ++i) pin(Fv[i]);
    TS(5);
    if (OP_TH) {
      if (w == 0) {
        const u32 go = OFF_ML + ((2 * cq * 128 + l) * 32 + h2) * 16;
        for (int it = 0; it < SPIN_MAX; ++it) {
          const u32 z = opq(go);
          const u32x4 g0 = wld16(R, z), g1 = wld16(R, z + 64 * 512);
          const int c = __builtin_popcountll(__ballot(g0[0] + g0[1] + E == g0[3])) + __builtin_popcountll(__ballot(g1[0] + g1[1] + E == g1[3]));
          if (c >= OP_TH) break;
          __builtin_amdgcn_s_sleep(OP_PS);
        }
      }
      bar();
    }
    TS(6);
#pragma unroll
    for (int i = 0; i < 4; ++i) o[i] = wld16(R, oo + (i & 1) * 1024 + (i >> 1) * 16384);
#pragma unroll
    for (int i = 0; i < 4; ++i) k[i] = wld16(R, kq + (i & 1) * 4096 + (i >> 1) * 65536);
    for (int it = 0; it < SPIN_MAX; ++it) {
      bool nb[4];
#pragma unroll
      for (int i = 0; i < 4; ++i) nb[i] = (sum8l(sum4(o[i])) + E != k[i][2]) | (k[i][0] + k[i][1] + E != k[i][3]);
      if (!__any(nb[0] | nb[1] | nb[2] | nb[3])) break;
      const u32 z = opq(0u);
#pragma unroll
      for (int i = 0; i < 4; ++i)
        if (nb[i]) { o[i] = wld16(R, oo + z + (i & 1) * 1024 + (i >> 1) * 16384); k[i] = wld16(R, kq + z + (i & 1) * 4096 + (i >> 1) * 65536); }
    }
    TS(9);
    float Mw = fmaxf(__uint_as_float(k[0][0]), __uint_as_float(k[1][0]));
    Mw = fmaxf(Mw, dpp<0x128>(Mw));
    Mw = x32_max(x16_max(Mw));
    const float w0 = __expf(__uint_as_float(k[0][0]) - Mw), w1 = __expf(__uint_as_float(k[1][0]) - Mw);
    float Lw = w0 * __uint_as_float(k[0][1]) + w1 * __uint_as_float(k[1][1]);
    Lw += dpp<0x128>(Lw);
    Lw = x32_sum(x16_sum(Lw));
    if (l == 0) *(LAS f32x2*)(sm + L_QB + w * 8) = (f32x2){Mw, Lw};
    float a[16];
#pragma unroll
    for (int i = 0; i < 4; ++i) {
      a[2 * i] = w0 * bflo(o[0][i]) + w1 * bflo(o[1][i]);
      a[2 * i + 1] = w0 * bfhi(o[0][i]) + w1 * bfhi(o[1][i]);
      a[8 + 2 * i] = w0 * bflo(o[2][i]) + w1 * bflo(o[3][i]);
      a[8 + 2 * i + 1] = w0 * bfhi(o[2][i]) + w1 * bfhi(o[3][i]);
    }
    float r8[8];
#pragma unroll
    for (int i = 0; i < 8; ++i) {
      auto sw = __builtin_amdgcn_permlane16_swap(__float_as_uint(a[i]), __float_as_uint(a[8 + i]), false, false);
      float t = __uint_as_float(sw[0]) + __uint_as_float(sw[1]);
      t += dpp<0x128>(t);
      r8[i] = x32_sum(t);
    }
    if (l < 32 && n < 8) {
      LAS f32x4* r = (LAS f32x4*)(sm + L_RED + w * 512 + g * 256 + n * 32);
      r[0] = (f32x4){r8[0], r8[1], r8[2], r8[3]}; r[1] = (f32x4){r8[4], r8[5], r8[6], r8[7]};
    }
    TS(11);
    bar();
    TS(12);
    if (w < 4) {
      const LAS f32x4* r = (const LAS f32x4*)(sm + L_RED + (n & 1) * 2048 + (w >> 1) * 256 + (16 * g + 8 * ((n >> 1) & 1)) * 4);
      const f32x4 e0 = *(const LAS f32x4*)(sm + L_QB + (n & 1) * 32), e1 = *(const LAS f32x4*)(sm + L_QB + (n & 1) * 32 + 16);
      const float M = fmaxf(fmaxf(e0[0], e0[2]), fmaxf(e1[0], e1[2]));
      float f0 = __expf(e0[0] - M), f1 = __expf(e0[2] - M), f2 = __expf(e1[0] - M), f3 = __expf(e1[2] - M);
      const float inv = rcp((f0 * e0[1] + f1 * e0[3]) + (f2 * e1[1] + f3 * e1[3]));
      f0 *= inv; f1 *= inv; f2 *= inv; f3 *= inv;
      f32x4 lo = (r[0] * f0 + r[32] * f1) + (r[64] * f2 + r[96] * f3), hi = (r[1] * f0 + r[33] * f1) + (r[65] * f2 + r[97] * f3);
      const u32x4 af = {pk2(lo[0], lo[1]), pk2(lo[2], lo[3]), pk2(hi[0], hi[1]), pk2(hi[2], hi[3])};
      float v0[8], v1[8];
#pragma unroll
      for (int i = 0; i < 8; ++i) {
        f32x4 c = mfma(af, Fv[i], Z4);
        float a0 = kg ? c[2] : c[0], a1 = kg ? c[3] : c[1];
        v0[i] = a0 + dpp<0x128>(a0);
        v1[i] = a1 + dpp<0x128>(a1);
      }
      if (l < 8) {
        LAS char* d = sm + L_HB + (w >> 1) * 512 + (w & 1) * 128 + l * 16;
        *(LAS u32x4*)d = (u32x4){pk2(v0[0], v0[1]), pk2(v0[2], v0[3]), pk2(v0[4], v0[5]), pk2(v0[6], v0[7])};
        *(LAS u32x4*)(d + 256) = (u32x4){pk2(v1[0], v1[1]), pk2(v1[2], v1[3]), pk2(v1[4], v1[5]), pk2(v1[6], v1[7])};
      }
    }
    bar();
    TS(13);
    {
      f32x4 acc[4] = {Z4, Z4, Z4, Z4};
#pragma unroll
      for (int ks = 0; ks < 4; ++ks) {
        u32x4 vf = *(const LAS u32x4*)(sm + L_HB + (n & 3) * 256 + ks * 64 + g * 16);
#pragma unroll
        for (int i = 0; i < 4; ++i) acc[i] = mfma(Fo[4 * ks + i], vf, acc[i]);
      }
      u32x4 ov;
#pragma unroll
      for (int r = 0; r < 4; ++r) {
        const float lo = (n & 4) ? acc[1][r] : acc[0][r], hi = (n & 4) ? acc[3][r] : acc[2][r];
        const float v = (n & 8) ? hi : lo;
        ov[r] = tg8(v + dpp<0x4E>(v), t8);
      }
      if (!(n & 2)) wst16(R, OFF_OUTP + (u32)((n & 1) * 16 + 2 * w + (n >> 3)) * 16384 + chunk * 128 + ((n >> 2) & 1) * 64 + g * 16, ov);
    }
    TS(10);
  } else if (chunk < 32) {
    const int b = chunk >> 4, m = (chunk & 15) * 32 + (l & 31);
    const u32 oo = OFF_OUTP + (u32)chunk * 16384 + w * 2048 + l * 16;
    const u32 go = OFF_MODG + (u32)xcd * 16384 + b * 2048 + m * 4;
    u32 ga = wld4(R, go), gb = wld4(R, go + 12288);
    const float xx = bflo(gld2(x + b * 512 + m));
    for (int it = 0; it < SPIN_MAX; ++it) {
      if (!__any((((ga ^ E) | (gb ^ E)) & 0xFFFFu) != 0u)) break;
      const u32 o = opq(go);
      ga = wld4(R, o); gb = wld4(R, o + 12288);
    }
    u32x4 v0 = wld16(R, oo), v1 = wld16(R, oo + 1024);
    for (int it = 0; it < SPIN_MAX; ++it) {
      const bool n0 = bad8(v0, E), n1 = bad8(v1, E);
      if (!__any(n0 | n1)) break;
      __builtin_amdgcn_s_sleep(1);
      const u32 o = opq(oo);
      if (n0) v0 = wld16(R, o);
      if (n1) v1 = wld16(R, o + 1024);
    }
    TS(11);
    f32x4 s4;
#pragma unroll
    for (int i = 0; i < 4; ++i) { float t = un8(v0[i]) + un8(v1[i]); t += dpp<0x128>(t); s4[i] = x32_sum(x16_sum(t)); }
    if (l < 8) *(LAS f32x4*)(sm + L_RED + w * 128 + l * 16) = s4;
    bar();
    const LAS float* r = (const LAS float*)(sm + L_RED + (l & 31) * 4);
    const float at = rb(((r[0] + r[32]) + (r[64] + r[96])) + ((r[128] + r[160]) + (r[192] + r[224])));
    const float x1 = rb(xx + rb(bfhi(ga) * at));
    if (l < 32) wst4(R, OFF_X1 + (u32)w * 4096 + b * 2048 + m * 4, tg16(x1, t16));
    if (chunk == 0 && tid == 0) wst4(R, 0, E);
    TS(12);
    const u32 fo = OFF_FIN + (u32)chunk * 8192 + w * 896 + (l < 56 ? l : l - 56) * 16;
    u32x4 f = wld16(R, fo);
    for (int it = 0; it < SPIN_MAX; ++it) {
      if (!__any(bad8(f, E))) break;
      __builtin_amdgcn_s_sleep(1);
      f = wld16(R, opq(fo));
    }
    TS(13);
#pragma unroll
    for (int i = 0; i < 4; ++i) { float t = l < 56 ? un8(f[i]) : 0.f; t += dpp<0x128>(t); s4[i] = x32_sum(x16_sum(t)); }
    if (l < 8) *(LAS f32x4*)(sm + L_RED + 1024 + w * 128 + l * 16) = s4;
    bar();
    if (w == 0) {
      const float y = rb(((r[256] + r[288]) + (r[320] + r[352])) + ((r[384] + r[416]) + (r[448] + r[480])));
      const float o = x1 + rb(bfhi(gb) * y);
      if (l < 32) *(GAS u16*)(out + b * 512 + m) = (u16)(pk2(o, o) & 0xFFFFu);
    }
    TS(14);
  } else if (chunk < 64) {
    const u32 pm = mod_cols(chunk - 16);
    const u32 o = OFF_MODG + (u32)w * 16384 + ((chunk - 32) >> 3) * 4096 + ((chunk & 7) * 64 + l) * 4;
    wst4(R, o, (pm << 16) | t16); wst4(R, o + 2048, (pm & 0xFFFF0000u) | t16);
    TS(10);
  } else if (fi >= 0) {
    u32x4 W[8], FG[8];
    {
      const u16* p = (colblk < 4 ? wg : wu) + (size_t)(64 * w + 32 * kg + 8 * g) * 1792 + fi * 32 + 8 * (colblk & 3);
#pragma unroll
      for (int i = 0; i < 8; ++i) W[i] = gld16(p + i * 1792);
    }
    u32x2 Wd[8];
    {
      const u16* p = wd + (size_t)(fi * 32 + 8 * g) * 512 + 64 * w + 4 * n;
#pragma unroll
      for (int i = 0; i < 8; ++i) Wd[i] = gl8<NT_W1>(p + i * 512);
    }
    sched_fence();
    u32x4 Fd[4];
    tr8x8(W, FG);
    tr8x4(Wd, Fd);
#pragma unroll
    for (int i = 0; i < 8; ++i) pin(FG[i]);
#pragma unroll
    for (int i = 0; i < 4; ++i) pin(Fd[i]);
    TS(10);
    if (w < 2) {
      const u32 so = OFF_MODG + (u32)xcd * 16384 + 4096 + (u32)w * 2048 + l * 16;
      u32x4 sb0 = wld16(R, so), sb1 = wld16(R, so + 1024), cb0 = wld16(R, so + 4096), cb1 = wld16(R, so + 5120);
      for (int it = 0; it < SPIN_MAX; ++it) {
        if (!__any(bad16(sb0, E) | bad16(sb1, E) | bad16(cb0, E) | bad16(cb1, E))) break;
        const u32 o = opq(so);
        sb0 = wld16(R, o); sb1 = wld16(R, o + 1024); cb0 = wld16(R, o + 4096); cb1 = wld16(R, o + 5120);
      }
      float c1[8], sh[8];
#pragma unroll
      for (int i = 0; i < 4; ++i) {
        c1[i] = rb(1.f + bfhi(cb0[i])); c1[4 + i] = rb(1.f + bfhi(cb1[i]));
        sh[i] = bfhi(sb0[i]); sh[4 + i] = bfhi(sb1[i]);
      }
#pragma unroll
      for (int i = 0; i < 8; ++i) { pin(c1[i]); pin(sh[i]); }
      TS(11);
      const u32 xo = OFF_X1 + (u32)xcd * 4096 + (u32)w * 2048 + l * 16;
      u32x4 a0 = wld16(R, xo), a1 = wld16(R, xo + 1024);
      for (int it = 0; it < SPIN_MAX; ++it) {
        if (!__any(bad16(a0, E) | bad16(a1, E))) break;
        const u32 o = opq(xo);
        a0 = wld16(R, o); a1 = wld16(R, o + 1024);
      }
      TS(12);
      float xv[8];
#pragma unroll
      for (int i = 0; i < 4; ++i) { xv[i] = bfhi(a0[i]); xv[4 + i] = bfhi(a1[i]); }
      const float m = wave_sum(((xv[0] + xv[1]) + (xv[2] + xv[3])) + ((xv[4] + xv[5]) + (xv[6] + xv[7]))) * (1.f / 512.f);
      float qq = 0.f;
#pragma unroll
      for (int i = 0; i < 8; ++i) { float d = xv[i] - m; qq += d * d; }
      const float var = wave_sum(qq) * (1.f / 512.f);
      const float mean = rb(m), sd = rcp(rb(__builtin_amdgcn_sqrtf(rb(rb(var) + 1e-5f))));
      float hv[8];
#pragma unroll
      for (int i = 0; i < 8; ++i) hv[i] = rb(rb(rb(xv[i] - mean) * sd) * c1[i]) + sh[i];
      *(LAS u32x2*)(sm + L_HB + w * 1024 + l * 8) = (u32x2){pk2(hv[0], hv[1]), pk2(hv[2], hv[3])};
      *(LAS u32x2*)(sm + L_HB + w * 1024 + 512 + l * 8) = (u32x2){pk2(hv[4], hv[5]), pk2(hv[6], hv[7])};
    }
    bar();
    {
      u32x4 af = *(const LAS u32x4*)(sm + L_HB + (n & 1) * 1024 + (64 * w + 32 * ((n >> 1) & 1) + 8 * g) * 2);
      float v0[8], v1[8];
#pragma unroll
      for (int i = 0; i < 8; ++i) {
        f32x4 a = mfma(af, FG[i], Z4);
        float a0 = kg ? a[2] : a[0], a1 = kg ? a[3] : a[1];
        v0[i] = a0 + dpp<0x128>(a0);
        v1[i] = a1 + dpp<0x128>(a1);
      }
      if (l < 8) {
        LAS f32x4* r = (LAS f32x4*)(sm + L_RED + 4096 + w * 512 + l * 32);
        r[0] = (f32x4){v0[0], v0[1], v0[2], v0[3]}; r[1] = (f32x4){v0[4], v0[5], v0[6], v0[7]};
        r[16] = (f32x4){v1[0], v1[1], v1[2], v1[3]}; r[17] = (f32x4){v1[4], v1[5], v1[6], v1[7]};
      }
    }
    bar();
    {
      const LAS float* r = (const LAS float*)(sm + L_RED + 4096 + (l >> 5) * 256 + (l & 31) * 4);
      float gs = ((r[0] + r[128]) + (r[256] + r[384])) + ((r[512] + r[640]) + (r[768] + r[896]));
      float us = ((r[32] + r[160]) + (r[288] + r[416])) + ((r[544] + r[672]) + (r[800] + r[928]));
      float hv = rb(gelu_f(rb(gs))) * rb(us);
      *(LAS u16*)(sm + L_P + w * 128 + l * 2) = (u16)(pk2(hv, hv) & 0xFFFFu);
      lds_wait();
      u32x4 ha = *(const LAS u32x4*)(sm + L_P + w * 128 + (n & 1) * 64 + g * 16);
      u32x4 o;
#pragma unroll
      for (int i = 0; i < 4; ++i) {
        f32x4 a = mfma(ha, Fd[i], Z4);
        o[i] = tg8((g & 1) ? a[1] : a[0], t8);
      }
      if (g < 2) wst16(R, OFF_FIN + (u32)(g * 16 + 2 * w + (n >> 3)) * 8192 + fi * 128 + (n & 7) * 16, o);
    }
    TS(13);
  }
  if (PF_CODE) asm volatile(".Lwc_b:");
}

extern "C" void kernel_launch(void* const* d_in, const int* in_sizes, int n_in, void* d_out, int out_size, void* d_ws,
                              size_t ws_size, hipStream_t stream) {
  (void)in_sizes; (void)n_in; (void)out_size; (void)ws_size;
  (void)hipFuncSetAttribute((const void*)fused_kernel, hipFuncAttributeMaxDynamicSharedMemorySize, (int)LDS_BYTES);
  hipLaunchKernelGGL(fused_kernel, dim3(256), dim3(512), LDS_BYTES, stream, (const u16*)d_in[0], (const u16*)d_in[1],
                     (const u16*)d_in[2], (const u16*)d_in[3], (const u16*)d_in[4], (const u16*)d_in[5], (const u16*)d_in[7],
                     (const u16*)d_in[8], (const u16*)d_in[9], (const u16*)d_in[10], (const u16*)d_in[11], (u16*)d_out, (char*)d_ws);
}
```

```cpp
#include <hip/hip_runtime.h>
#include <hip/hip_bf16.h>
#include <stdint.h>
#include <type_traits>

typedef unsigned short u16;
typedef unsigned int u32;
typedef __attribute__((ext_vector_type(4))) u32 u32x4;
typedef __attribute__((ext_vector_type(2))) u32 u32x2;
typedef __attribute__((ext_vector_type(4))) float f32x4;
typedef __attribute__((ext_vector_type(2))) float f32x2;
typedef __attribute__((ext_vector_type(8))) __bf16 bf16x8;
typedef __attribute__((ext_vector_type(2))) __bf16 bf16x2;
typedef __attribute__((ext_vector_type(4))) short s16x4;
#define LAS __attribute__((address_space(3)))
#define GAS __attribute__((address_space(1)))
#define DI __device__ __forceinline__

constexpr u32 OFF_HINT = 4096;
constexpr u32 OFF_HM = OFF_HINT + 16384;
constexpr u32 OFF_MODG = OFF_HM + 65536;
constexpr u32 OFF_X1 = OFF_MODG + 131072;
constexpr u32 OFF_QA = OFF_X1 + 32768;
constexpr u32 OFF_QK = OFF_QA + 131072;
constexpr u32 OFF_OP = OFF_QK + 65536;
constexpr u32 OFF_ML = OFF_OP + 4194304;
constexpr u32 OFF_OUTP = OFF_ML + 524288;
constexpr u32 OFF_FIN = OFF_OUTP + 524288;
constexpr u32 WS_BYTES = OFF_FIN + 262144;
constexpr u32 OFF_DBG = 8u << 20;
static_assert(WS_BYTES <= OFF_DBG, "ws");
constexpr int SPIN_MAX = 1 << 16;

constexpr u32 L_C = 0;
constexpr u32 L_Q = 0;
constexpr u32 L_P = 32768;
constexpr u32 L_MX = L_P + 8704;
constexpr u32 L_LS = L_MX + 1024;
constexpr u32 L_RED = 131072;
constexpr u32 L_HB = L_RED + 8192;
constexpr u32 L_QB = L_HB + 2048;
constexpr u32 L_DUM = L_QB + 512;
constexpr u32 LDS_BYTES = L_DUM + 256;
static_assert(LDS_BYTES <= 163840, "lds");

DI float bflo(u32 v) { return __builtin_bit_cast(float, v << 16); }
DI float bfhi(u32 v) { return __builtin_bit_cast(float, v & 0xFFFF0000u); }
DI u32 pk2(float a, float b) {
  f32x2 f = {a, b};
  return __builtin_bit_cast(u32, __builtin_convertvector(f, bf16x2));
}
DI float rb(float a) { return bflo(pk2(a, a)); }
DI f32x4 mfma(u32x4 a, u32x4 b, f32x4 c) {
  return __builtin_amdgcn_mfma_f32_16x16x32_bf16(__builtin_bit_cast(bf16x8, a), __builtin_bit_cast(bf16x8, b), c, 0, 0, 0);
}
template <int CTRL> DI float dpp(float x) {
  return __builtin_bit_cast(float, __builtin_amdgcn_update_dpp(0, __builtin_bit_cast(int, x), CTRL, 0xF, 0xF, true));
}
DI float x16_sum(float x) { auto s = __builtin_amdgcn_permlane16_swap(__float_as_uint(x), __float_as_uint(x), false, false); return __uint_as_float(s[0]) + __uint_as_float(s[1]); }
DI float x32_sum(float x) { auto s = __builtin_amdgcn_permlane32_swap(__float_as_uint(x), __float_as_uint(x), false, false); return __uint_as_float(s[0]) + __uint_as_float(s[1]); }
DI float x16_max(float x) { auto s = __builtin_amdgcn_permlane16_swap(__float_as_uint(x), __float_as_uint(x), false, false); return fmaxf(__uint_as_float(s[0]), __uint_as_float(s[1])); }
DI float x32_max(float x) { auto s = __builtin_amdgcn_permlane32_swap(__float_as_uint(x), __float_as_uint(x), false, false); return fmaxf(__uint_as_float(s[0]), __uint_as_float(s[1])); }
DI float wave_sum(float x) {
  x += dpp<0xB1>(x); x += dpp<0x4E>(x); x += dpp<0x141>(x); x += dpp<0x140>(x);
  return x32_sum(x16_sum(x));
}
DI float wave_max(float x) {
  x = fmaxf(x, dpp<0xB1>(x)); x = fmaxf(x, dpp<0x4E>(x)); x = fmaxf(x, dpp<0x141>(x)); x = fmaxf(x, dpp<0x140>(x));
  return x32_max(x16_max(x));
}
DI float silu_f(float x) { return x * __builtin_amdgcn_rcpf(1.f + __expf(-x)); }
DI float gelu_f(float g) {
  float z = 0.7978845608028654f * (g + 0.044715f * g * g * g);
  float e = __expf(2.f * z);
  float t = 1.f - 2.f * __builtin_amdgcn_rcpf(e + 1.f);
  return 0.5f * g * (1.f + t);
}
DI u32x4 gld16(const void* p) { return *(const GAS u32x4*)p; }
DI u32x2 gld8(const void* p) { return *(const GAS u32x2*)p; }
DI u32 gld2(const void* p) { return *(const GAS u16*)p; }
typedef __amdgpu_buffer_rsrc_t rsrc_t;
DI u32x4 wld16(rsrc_t r, u32 off) { return __builtin_amdgcn_raw_buffer_load_b128(r, off, 0, 16); }
DI u32x2 wld8(rsrc_t r, u32 off) { return __builtin_amdgcn_raw_buffer_load_b64(r, off, 0, 16); }
DI u32 wld4(rsrc_t r, u32 off) { return __builtin_amdgcn_raw_buffer_load_b32(r, off, 0, 16); }
DI void wst16(rsrc_t r, u32 off, u32x4 v) { __builtin_amdgcn_raw_buffer_store_b128(v, r, off, 0, 16); }
DI void wst8(rsrc_t r, u32 off, u32x2 v) { __builtin_amdgcn_raw_buffer_store_b64(v, r, off, 0, 16); }
DI void wst4(rsrc_t r, u32 off, u32 v) { __builtin_amdgcn_raw_buffer_store_b32(v, r, off, 0, 16); }
DI u32 opq(u32 v) { asm volatile("" : "+v"(v)); return v; }
template <class T> DI void pin(T& v) { asm volatile("" : "+v"(v)); }
DI float rcp(float x) { return __builtin_amdgcn_rcpf(x); }
DI void fence_c() { asm volatile("" ::: "memory"); }
DI void sched_fence() { __builtin_amdgcn_sched_barrier(0); }
DI void lds_wait() { asm volatile("s_waitcnt lgkmcnt(0)" ::: "memory"); }
DI void bar() { asm volatile("s_waitcnt lgkmcnt(0)\n\ts_barrier" ::: "memory"); }
DI void tr8x8(const u32x4 (&W)[8], u32x4 (&F)[8]) {
#pragma unroll
  for (int i = 0; i < 8; ++i)
#pragma unroll
    for (int p = 0; p < 4; ++p)
      F[i][p] = __builtin_amdgcn_perm(W[2 * p + 1][i >> 1], W[2 * p][i >> 1], (i & 1) ? 0x07060302u : 0x05040100u);
}
DI void tr8x4(const u32x2* W, u32x4* F) {
#pragma unroll
  for (int i = 0; i < 4; ++i)
#pragma unroll
    for (int p = 0; p < 4; ++p)
      F[i][p] = __builtin_amdgcn_perm(W[2 * p + 1][i >> 1], W[2 * p][i >> 1], (i & 1) ? 0x07060302u : 0x05040100u);
}
DI void ln_stats(u32x4 v, float& mean, float& sd) {
  float s = 0.f;
#pragma unroll
  for (int i = 0; i < 4; ++i) s += bflo(v[i]) + bfhi(v[i]);
  float m = wave_sum(s) * (1.f / 512.f);
  float q = 0.f;
#pragma unroll
  for (int i = 0; i < 4; ++i) { float a = bflo(v[i]) - m, b = bfhi(v[i]) - m; q += a * a + b * b; }
  float var = wave_sum(q) * (1.f / 512.f);
  mean = rb(m);
  sd = __builtin_amdgcn_rcpf(rb(__builtin_amdgcn_sqrtf(rb(rb(var) + 1e-5f))));
}
DI float ln_mod(float x, float mean, float sd, float sc, float sh) {
  float ln = rb(rb(x - mean) * sd);
  return rb(rb(ln * rb(1.f + sc)) + sh);
}

#define PF_TLB 1
#define PF_CODE 1
#define PF_LATE 1
#define PF_C 1
#define PF_NW 6
#define NT_WQ 1
#define NT_CB 1
#define NT_CA 0
#define NT_W1 1
#define HINT_SLEEP 40
#define B_DELAY 60
#define OP_SLEEP 0
#define B_GATE 0
#define A_EARLY 0
#define ROT 0
#define OP_TH 64
#define OP_PS 4
#define WO_EARLY 1
#define NT_WO 1
template <int NT> DI u32x4 gl16(const void* p) {
  if (NT) return __builtin_nontemporal_load((const GAS u32x4*)p);
  return *(const GAS u32x4*)p;
}
template <int NT> DI u32x2 gl8(const void* p) {
  if (NT) return __builtin_nontemporal_load((const GAS u32x2*)p);
  return *(const GAS u32x2*)p;
}
template <int NT> DI void dma16(const void* gp, u32 lds) {
  u32 keep;
  if (NT) asm volatile("s_mov_b32 %0, m0\n\ts_mov_b32 m0, %2\n\ts_nop 0\n\tglobal_load_lds_dwordx4 %1, off nt\n\ts_mov_b32 m0, %0" : "=&s"(keep) : "v"(gp), "s"(lds) : "memory");
  else asm volatile("s_mov_b32 %0, m0\n\ts_mov_b32 m0, %2\n\ts_nop 0\n\tglobal_load_lds_dwordx4 %1, off\n\ts_mov_b32 m0, %0" : "=&s"(keep) : "v"(gp), "s"(lds) : "memory");
}
template <int SC> DI void touch4(const void* gp, u32 lds) {
  u32 keep;
  if (SC) asm volatile("s_mov_b32 %0, m0\n\ts_mov_b32 m0, %2\n\ts_nop 0\n\tglobal_load_lds_dword %1, off sc1\n\ts_mov_b32 m0, %0" : "=&s"(keep) : "v"(gp), "s"(lds) : "memory");
  else asm volatile("s_mov_b32 %0, m0\n\ts_mov_b32 m0, %2\n\ts_nop 0\n\tglobal_load_lds_dword %1, off\n\ts_mov_b32 m0, %0" : "=&s"(keep) : "v"(gp), "s"(lds) : "memory");
}
template <int CTRL> DI u32 dppu(u32 x) { return (u32)__builtin_amdgcn_update_dpp(0, (int)x, CTRL, 0xF, 0xF, true); }
DI u32 sum8l(u32 x) { x += dppu<0xB1>(x); x += dppu<0x4E>(x); x += dppu<0x141>(x); return x; }
DI u32 sum16l(u32 x) { x = sum8l(x); x += dppu<0x140>(x); return x; }
DI bool bad16x2(u32x2 v, u32 t) { return (((v[0] ^ t) | (v[1] ^ t)) & 0xFFFFu) != 0u; }
DI void vm_wait0() { asm volatile("s_waitcnt vmcnt(0)" ::: "memory"); }
DI u32 sum4(u32x4 v) { return (v[0] + v[1]) + (v[2] + v[3]); }
DI bool bad16(u32x4 v, u32 t) { return (((v[0] ^ t) | (v[1] ^ t) | (v[2] ^ t) | (v[3] ^ t)) & 0xFFFFu) != 0u; }
DI bool bad8(u32x4 v, u32 t) { return (((v[0] ^ t) | (v[1] ^ t) | (v[2] ^ t) | (v[3] ^ t)) & 0xFFu) != 0u; }
DI u32 tg16(float bfval, u32 t16) { return (__float_as_uint(bfval) & 0xFFFF0000u) | t16; }
DI u32 tg8(float v, u32 t8) { return (__float_as_uint(v) & 0xFFFFFF00u) | t8; }
DI float un8(u32 v) { return __uint_as_float(v & 0xFFFFFF00u); }

#define TS(i) do {} while (0)

__global__ void __launch_bounds__(512, 2)
fused_kernel(const u16* __restrict__ x, const u16* __restrict__ ckv, const u16* __restrict__ wq, const u16* __restrict__ wuk,
             const u16* __restrict__ wuv, const u16* __restrict__ wo, const u16* __restrict__ wg, const u16* __restrict__ wu,
             const u16* __restrict__ wd, const u16* __restrict__ cnd, const u16* __restrict__ wmod, u16* __restrict__ out,
             char* ws) {
  extern __shared__ __attribute__((aligned(16))) char smem_raw[];
  LAS char* sm = (LAS char*)smem_raw;
  const u32 lds0 = (u32)(size_t)sm;
  const int tid = threadIdx.x, l = tid & 63;
  const int w = __builtin_amdgcn_readfirstlane(tid >> 6);
  const int n = l & 15, g = l >> 4;
  const int bid = blockIdx.x, xcd = bid & 7, j = bid >> 3;
  const int cls = (j >= 2 && j < 18) ? 0 : 1, pairidx = j < 2 ? j : (j < 18 ? j - 2 : j - 16);
  const int chunk = xcd + 8 * pairidx;
  const int h2 = 4 * xcd + (pairidx >> 2), cq = pairidx & 3;
  const int fi = chunk - 72;
  const rsrc_t R = __builtin_amdgcn_make_buffer_rsrc(ws, 0, WS_BYTES, 0x00020000);
  const f32x4 Z4 = {0.f, 0.f, 0.f, 0.f};
  TS(0);
  const u32 Ev = *(const GAS u32*)ws + 1u;
  const int colblk = n & 7, kg = n >> 3;
  u32x4 Wv[8];
  const int rot = (pairidx + 2 * w) & 15;
  auto load_rows = [&](const u16* src, int r0, auto nr, auto nt, int ib = 0) {
#pragma unroll
    for (int i = 0; i < decltype(nr)::value; ++i) {
      if (ROT && decltype(nr)::value == 16) {
        const int ii = (i + rot) & 15;
        const u32 swz = ((ii & 3) << 2) | (ii >> 2);
        dma16<decltype(nt)::value>(src + (size_t)(r0 + ii) * 512 + (l ^ swz) * 8, lds0 + L_C + (r0 + ii) * 1024);
      } else {
        const u32 swz = (((i + ib) & 3) << 2) | (((i + ib) >> 2) & 3);
        dma16<decltype(nt)::value>(src + (size_t)(r0 + i) * 512 + (l ^ swz) * 8, lds0 + L_C + (r0 + i) * 1024);
      }
    }
  };
  auto read_tr = [&](u32x4 (&f)[16]) {
    const int q = n >> 2, pp = n & 3;
#pragma unroll
    for (int ks = 0; ks < 4; ++ks)
#pragma unroll
      for (int t = 0; t < 4; ++t)
#pragma unroll
        for (int tt = 0; tt < 2; ++tt) {
          const int row = 32 * ks + 8 * g + 4 * tt + q;
          const u32 swz = (q << 2) | ((2 * g + tt) & 3);
          const u32 ad = L_C + row * 1024 + (((8 * w + 2 * t + (pp >> 1)) ^ swz) * 16) + 8 * (pp & 1);
          u32x2 v = __builtin_bit_cast(u32x2, __builtin_amdgcn_ds_read_tr16_b64_v4i16((LAS s16x4*)(sm + ad)));
          f[4 * ks + t][2 * tt] = v[0]; f[4 * ks + t][2 * tt + 1] = v[1];
        }
  };
  auto mod_cols = [&](int cb) -> u32 {
    u32x4 cfr = gld16(cnd + (n & 1) * 512 + 64 * w + 32 * ((n >> 1) & 1) + 8 * g);
    u32x4 W[8];
    const u16* p = wmod + (size_t)(64 * w + 32 * kg + 8 * g) * 3072 + cb * 64 + 8 * colblk;
#pragma unroll
    for (int i = 0; i < 8; ++i) W[i] = gl16<NT_W1>(p + i * 3072);
    sched_fence();
    u32x4 af;
#pragma unroll
    for (int i = 0; i < 4; ++i) af[i] = pk2(silu_f(bflo(cfr[i])), silu_f(bfhi(cfr[i])));
    pin(af);
    u32x4 F[8];
    tr8x8(W, F);
    float v0[8], v1[8];
#pragma unroll
    for (int i = 0; i < 8; ++i) {
      f32x4 a = mfma(af, F[i], Z4);
      float a0 = kg ? a[2] : a[0], a1 = kg ? a[3] : a[1];
      v0[i] = a0 + dpp<0x128>(a0);
      v1[i] = a1 + dpp<0x128>(a1);
    }
    if (l < 8) {
      LAS f32x4* r = (LAS f32x4*)(sm + L_RED + w * 512 + l * 32);
      r[0] = (f32x4){v0[0], v0[1], v0[2], v0[3]}; r[1] = (f32x4){v0[4], v0[5], v0[6], v0[7]};
      r[16] = (f32x4){v1[0], v1[1], v1[2], v1[3]}; r[17] = (f32x4){v1[4], v1[5], v1[6], v1[7]};
    }
    bar();
    const LAS float* r = (const LAS float*)(sm + L_RED + l * 4);
    float m0 = ((r[0] + r[128]) + (r[256] + r[384])) + ((r[512] + r[640]) + (r[768] + r[896]));
    float m1 = ((r[64] + r[192]) + (r[320] + r[448])) + ((r[576] + r[704]) + (r[832] + r[960]));
    return pk2(m0, m1);
  };
  typedef std::integral_constant<int, 16> I16;
  typedef std::integral_constant<int, 32> I32;
  const u16* crow = ckv + (size_t)chunk * 128 * 512;
  const u16* worow = wo + (size_t)h2 * 128 * 512;
  u32x4 cf[16], ca[16];
  auto read_frags = [&]() {
    const u32 swz = ((n & 3) << 2) | (n >> 2);
#pragma unroll
    for (int ks = 0; ks < 16; ++ks) cf[ks] = *(const LAS u32x4*)(sm + L_C + (16 * w + n) * 1024 + (((4 * ks + g) ^ swz) * 16));
    read_tr(ca);
#pragma unroll
    for (int ks = 0; ks < 16; ++ks) { pin(cf[ks]); pin(ca[ks]); }
  };
  auto load_s4 = [&]() {
    if (w < 4) {
      const u16* p = wuv + (size_t)(cq * 128 + (w >> 1) * 64 + 32 * kg + 4 * g) * 4096 + h2 * 128 + 64 * (w & 1) + 8 * colblk;
#pragma unroll
      for (int i = 0; i < 8; ++i) Wv[i] = gl16<NT_W1>(p + (size_t)(16 * (i >> 2) + (i & 3)) * 4096);
    }
    load_rows(worow, 16 * w, I16(), std::integral_constant<int, NT_WO>());
  };
  auto hint_wait = [&](u32 e) {
    if (w == 0) {
      const u32 ho = OFF_HINT + (u32)xcd * 2048 + n * 128;
      for (int it = 0; it < SPIN_MAX; ++it) {
        if (!__any(wld4(R, opq(ho)) != e)) break;
        __builtin_amdgcn_s_sleep(2);
      }
      if (HINT_SLEEP) __builtin_amdgcn_s_sleep(HINT_SLEEP);
    }
    bar();
  };
  auto warm_pages = [&]() {
    if (PF_TLB && w == 7) {
      const char* pa = ws + min((u32)l * 131072u, WS_BYTES - 128u);
      if (l == 63) pa = (const char*)out;
      if (l == 62) pa = (const char*)(wuv + (size_t)cq * 128 * 4096);
      if (l == 61) pa = (const char*)(wuv + (size_t)(cq * 128 + 127) * 4096);
      if (l == 60) pa = (const char*)worow;
      if (l == 59) pa = (const char*)wg;
      if (l == 58) pa = (const char*)(wg + 512 * 1792 - 64);
      if (l == 57) pa = (const char*)wu;
      if (l == 56) pa = (const char*)(wu + 512 * 1792 - 64);
      if (l == 55) pa = (const char*)wd;
      if (l == 54) pa = (const char*)(wd + 512 * 1792 - 64);
      if (l == 53) pa = (const char*)x;
      touch4<1>(pa, lds0 + L_DUM);
    }
  };
  auto warm_c = [&]() {
    if (PF_C && w == 6) touch4<1>((l & 4) ? ckv + (size_t)(xcd + 8 * (l & 1)) * 65536 : crow + (l & 3) * 16384, lds0 + L_DUM);
  };
  auto warm_code = [&]() {
    if (PF_CODE) {
      unsigned long long pc; u32 len;
      asm volatile("s_getpc_b64 %0\n.Lwc_a:\n\ts_mov_b32 %1, .Lwc_b-.Lwc_a" : "=s"(pc), "=s"(len));
      touch4<0>((const char*)pc + min((u32)tid * 128u, len - 4u), lds0 + L_DUM);
    }
  };
  const u32 rb_ = (u32)(xcd & 1) * 2 + (chunk >> 6);
  u32 E;

  if (cls == 0) {
    u32x4 xv0 = gld16(x + 8 * l), xv1 = gld16(x + 512 + 8 * l);
    const u32 xm0 = *(const GAS u32*)(x + 2 * (tid & 255)), xm1 = *(const GAS u32*)(x + 512 + 2 * (tid & 255));
    u32x4 W[16];
    {
      const u16* p = wq + (size_t)(64 * w + 8 * g) * 4096 + h2 * 128 + 8 * n;
#pragma unroll
      for (int i = 0; i < 16; ++i) W[i] = gl16<NT_WQ>(p + (size_t)(32 * (i >> 3) + (i & 7)) * 4096);
    }
#pragma unroll
    for (int i = 0; i < 4; ++i)
      dma16<NT_W1>(wuk + (size_t)(128 * cq + 16 * w + 4 * i + g) * 4096 + h2 * 128 + (n ^ (4 * i + g)) * 8, lds0 + L_C + w * 16384 + 12288 + i * 1024);
    if (!PF_LATE) warm_pages();
    sched_fence();
    E = __builtin_amdgcn_readfirstlane(Ev);
    float mean0, sd0, mean1, sd1;
    ln_stats(xv0, mean0, sd0);
    ln_stats(xv1, mean1, sd1);
    float ln00 = rb(rb(bflo(xm0) - mean0) * sd0), ln01 = rb(rb(bflo(xm1) - mean1) * sd1);
    float ln10 = rb(rb(bfhi(xm0) - mean0) * sd0), ln11 = rb(rb(bfhi(xm1) - mean1) * sd1);
    u32x4 F[16];
    tr8x8(*(const u32x4(*)[8])(W), *(u32x4(*)[8])(F));
    tr8x8(*(const u32x4(*)[8])(W + 8), *(u32x4(*)[8])(F + 8));
#pragma unroll
    for (int i = 0; i < 16; ++i) pin(F[i]);
    pin(ln00); pin(ln01); pin(ln10); pin(ln11);
    vm_wait0();
    u32x4 uk[4];
#pragma unroll
    for (int ks = 0; ks < 4; ++ks) uk[ks] = *(const LAS u32x4*)(sm + L_C + w * 16384 + 12288 + n * 256 + (((4 * ks + g) ^ n) * 16));
#pragma unroll
    for (int i = 0; i < 4; ++i) pin(uk[i]);
    TS(1);
    if (w < 4) {
      const u32 ho = OFF_HM + (u32)xcd * 8192 + tid * 16;
      u32x4 hs = wld16(R, ho), hc = wld16(R, ho + 4096);
      for (int it = 0; it < SPIN_MAX; ++it) {
        if (!__any(bad16(hs, E) | bad16(hc, E))) break;
        const u32 o = opq(ho);
        hs = wld16(R, o); hc = wld16(R, o + 4096);
      }
      const float h00 = rb(ln00 * rb(1.f + bfhi(hc[0]))) + bfhi(hs[0]), h01 = rb(ln01 * rb(1.f + bfhi(hc[1]))) + bfhi(hs[1]);
      const float h10 = rb(ln10 * rb(1.f + bfhi(hc[2]))) + bfhi(hs[2]), h11 = rb(ln11 * rb(1.f + bfhi(hc[3]))) + bfhi(hs[3]);
      *(LAS u32*)(sm + L_HB + tid * 4) = pk2(h00, h10);
      *(LAS u32*)(sm + L_HB + 1024 + tid * 4) = pk2(h01, h11);
    }
    TS(2);
    bar();
    {
      u32x4 a0 = *(const LAS u32x4*)(sm + L_HB + (n & 1) * 1024 + w * 128 + g * 16);
      u32x4 a1 = *(const LAS u32x4*)(sm + L_HB + (n & 1) * 1024 + w * 128 + 64 + g * 16);
      f32x4 acc[8];
#pragma unroll
      for (int i = 0; i < 8; ++i) acc[i] = mfma(a0, F[i], Z4);
#pragma unroll
      for (int i = 0; i < 8; ++i) acc[i] = mfma(a1, F[8 + i], acc[i]);
      if (g == 0) {
        LAS f32x4* r = (LAS f32x4*)(sm + L_RED + w * 1024 + n * 32);
        r[0] = (f32x4){acc[0][0], acc[1][0], acc[2][0], acc[3][0]}; r[1] = (f32x4){acc[4][0], acc[5][0], acc[6][0], acc[7][0]};
        r[32] = (f32x4){acc[0][1], acc[1][1], acc[2][1], acc[3][1]}; r[33] = (f32x4){acc[4][1], acc[5][1], acc[6][1], acc[7][1]};
      }
    }
    bar();
    if (w < 4) {
      const LAS float* r = (const LAS float*)(sm + L_RED + tid * 4);
      float s = ((r[0] + r[256]) + (r[512] + r[768])) + ((r[1024] + r[1280]) + (r[1536] + r[1792]));
      *(LAS u16*)(sm + L_QB + tid * 2) = (u16)(pk2(s, s) & 0xFFFFu);
    }
    bar();
    {
      u32x4 qb[4];
#pragma unroll
      for (int ks = 0; ks < 4; ++ks) qb[ks] = *(const LAS u32x4*)(sm + L_QB + (n & 1) * 256 + ks * 64 + g * 16);
      f32x4 d0 = mfma(uk[0], qb[0], Z4), d1 = mfma(uk[1], qb[1], Z4);
      d0 = mfma(uk[2], qb[2], d0); d1 = mfma(uk[3], qb[3], d1);
      d0 += d1;
      if (n < 2) *(LAS u32x2*)(sm + L_HB + n * 256 + w * 32 + g * 8) = (u32x2){pk2(d0[0], d0[1]), pk2(d0[2], d0[3])};
    }
    bar();
    if (w == 0) {
      const u32x4 pay = *(const LAS u32x4*)(sm + L_HB + (g & 1) * 256 + n * 16);
      wst16(R, OFF_QA + (u32)(g * 32 + h2) * 1024 + cq * 256 + n * 16, pay);
      const u32 ck = sum16l(sum4(pay)) + E;
      if (n < 8) wst16(R, OFF_QK + (u32)((g * 32 + h2) * 4 + cq) * 128 + n * 16, (u32x4){ck, ck, ck, ck});
    }
    TS(3);
    if (PF_LATE) warm_pages();
    load_s4();
  } else {
    if (chunk < 16) {
      const u32 pm = mod_cols(chunk);
      TS(1);
      E = __builtin_amdgcn_readfirstlane(Ev);
      const u32 tt16 = E & 0xFFFFu;
      wst8(R, OFF_HM + (u32)w * 8192 + (chunk >> 3) * 4096 + ((chunk & 7) * 64 + l) * 8, (u32x2){(pm << 16) | tt16, (pm & 0xFFFF0000u) | tt16});
      if (l < 8) wst16(R, OFF_HINT + (u32)w * 2048 + chunk * 128 + l * 16, (u32x4){E, E, E, E});
      TS(2);
      warm_pages();
    } else {
      if (pairidx < PF_NW) warm_code();
      warm_c();
      if (!PF_LATE) warm_pages();
      if (B_DELAY) __builtin_amdgcn_s_sleep(B_DELAY);
      if (PF_LATE) warm_pages();
    }
    load_rows(crow, 16 * w, I16(), std::integral_constant<int, NT_CB>());
    E = __builtin_amdgcn_readfirstlane(Ev);
    vm_wait0();
    bar();
    TS(3);
    read_frags();
    TS(4);
    hint_wait(E);
    {
      const u32 qo = OFF_QA + (rb_ * 32 + 4 * w) * 1024 + l * 16, ko = OFF_QK + ((rb_ * 32 + 4 * w + (n & 3)) * 4 + g) * 128;
      u32x4 q[4];
#pragma unroll
      for (int i = 0; i < 4; ++i) q[i] = wld16(R, qo + i * 1024);
      u32 qk = wld4(R, ko);
      for (int it = 0; it < SPIN_MAX; ++it) {
        const u32 c0 = sum16l(sum4(q[0])), c1 = sum16l(sum4(q[1])), c2 = sum16l(sum4(q[2])), c3 = sum16l(sum4(q[3]));
        const u32 cs = (n & 2) ? ((n & 1) ? c3 : c2) : ((n & 1) ? c1 : c0);
        const unsigned long long bad = __ballot(cs + E != qk);
        if (!bad) break;
        const u32 z = opq(0u);
#pragma unroll
        for (int i = 0; i < 4; ++i)
          if (bad & (0x1111111111111111ull << i)) q[i] = wld16(R, qo + z + i * 1024);
        qk = wld4(R, ko + z);
      }
#pragma unroll
      for (int i = 0; i < 4; ++i) *(LAS u32x4*)(sm + L_Q + (4 * w + i) * 1024 + (l ^ ((i << 2) | (w & 3))) * 16) = q[i];
    }
    TS(5);
    sched_fence();
    bar();
    TS(6);
    f32x4 sa[2];
    {
      const u32 qswz = ((n & 3) << 2) | (n >> 2);
      u32x4 qf[2][8];
#pragma unroll
      for (int u = 0; u < 2; ++u)
#pragma unroll
        for (int ks = 0; ks < 8; ++ks) qf[u][ks] = *(const LAS u32x4*)(sm + L_Q + n * 1024 + ((4 * (8 * u + ks) + g) ^ qswz) * 16);
      sched_fence();
#pragma unroll
      for (int hh = 0; hh < 2; ++hh) {
        f32x4 s0 = Z4, s1 = Z4, s2 = Z4, s3 = Z4;
#pragma unroll
        for (int u = 0; u < 2; ++u) {
#pragma unroll
          for (int ks = 0; ks < 8; ks += 4) {
            s0 = mfma(cf[8 * u + ks], qf[u][ks], s0); s1 = mfma(cf[8 * u + ks + 1], qf[u][ks + 1], s1);
            s2 = mfma(cf[8 * u + ks + 2], qf[u][ks + 2], s2); s3 = mfma(cf[8 * u + ks + 3], qf[u][ks + 3], s3);
          }
          if (hh == 0) {
#pragma unroll
            for (int ks = 0; ks < 8; ++ks) qf[u][ks] = *(const LAS u32x4*)(sm + L_Q + (16 + n) * 1024 + ((4 * (8 * u + ks) + g) ^ qswz) * 16);
          }
          sched_fence();
        }
        sa[hh] = (s0 + s1) + (s2 + s3);
      }
    }
    float sc[2][4], mx[2];
#pragma unroll
    for (int hh = 0; hh < 2; ++hh) {
#pragma unroll
      for (int r = 0; r < 4; ++r) sc[hh][r] = rb(rb(sa[hh][r]) * 0.08838834764831845f);
      mx[hh] = x32_max(x16_max(fmaxf(fmaxf(sc[hh][0], sc[hh][1]), fmaxf(sc[hh][2], sc[hh][3]))));
    }
    if (g < 2) *(LAS float*)(sm + L_MX + (16 * g + n) * 32 + w * 4) = g ? mx[1] : mx[0];
    TS(9);
    bar();
    TS(15);
#pragma unroll
    for (int hh = 0; hh < 2; ++hh) {
      f32x4 m0 = *(const LAS f32x4*)(sm + L_MX + (16 * hh + n) * 32), m1 = *(const LAS f32x4*)(sm + L_MX + (16 * hh + n) * 32 + 16);
      mx[hh] = fmaxf(fmaxf(fmaxf(m0[0], m0[1]), fmaxf(m0[2], m0[3])), fmaxf(fmaxf(m1[0], m1[1]), fmaxf(m1[2], m1[3])));
    }
    float ls[2];
#pragma unroll
    for (int hh = 0; hh < 2; ++hh) {
      float p[4];
#pragma unroll
      for (int r = 0; r < 4; ++r) p[r] = __expf(sc[hh][r] - mx[hh]);
      *(LAS u32x2*)(sm + L_P + (16 * hh + n) * 272 + (16 * w + 4 * g) * 2) = (u32x2){pk2(p[0], p[1]), pk2(p[2], p[3])};
      ls[hh] = x32_sum(x16_sum((p[0] + p[1]) + (p[2] + p[3])));
    }
    if (g < 2) *(LAS float*)(sm + L_LS + (16 * g + n) * 32 + w * 4) = g ? ls[1] : ls[0];
    bar();
    TS(7);
#pragma unroll
    for (int hh = 0; hh < 2; ++hh) {
      f32x4 o[4] = {Z4, Z4, Z4, Z4};
#pragma unroll
      for (int ks = 0; ks < 4; ++ks) {
        u32x4 pf = *(const LAS u32x4*)(sm + L_P + (16 * hh + n) * 272 + (32 * ks + 8 * g) * 2);
#pragma unroll
        for (int t = 0; t < 4; ++t) o[t] = mfma(ca[4 * ks + t], pf, o[t]);
      }
      f32x4 l0 = *(const LAS f32x4*)(sm + L_LS + (16 * hh + n) * 32), l1 = *(const LAS f32x4*)(sm + L_LS + (16 * hh + n) * 32 + 16);
      const u32x4 pa = {pk2(o[0][0], o[0][1]), pk2(o[0][2], o[0][3]), pk2(o[1][0], o[1][1]), pk2(o[1][2], o[1][3])};
      const u32x4 pb = {pk2(o[2][0], o[2][1]), pk2(o[2][2], o[2][3]), pk2(o[3][0], o[3][1]), pk2(o[3][2], o[3][3])};
      u32x4 px;
#pragma unroll
      for (int i = 0; i < 4; ++i) px[i] = dppu<0x128>(pb[i]);
      const u32 r0 = OFF_OP + (u32)(((16 * hh + (n & 7)) * 8 + w) * 128 + chunk) * 128 + g * 32;
      wst16(R, r0 + (n & 8) * 2, (n & 8) ? px : pa);
      wst16(R, r0 + 8 * 8 * 128 * 128 + 16 - (n & 8) * 2, (n & 8) ? pa : px);
      u32 rs = sum4(pa) + sum4(pb);
      { auto t = __builtin_amdgcn_permlane16_swap(rs, rs, false, false); rs = t[0] + t[1]; }
      { auto t = __builtin_amdgcn_permlane32_swap(rs, rs, false, false); rs = t[0] + t[1]; }
      const float lt = ((l0[0] + l0[1]) + (l0[2] + l0[3])) + ((l1[0] + l1[1]) + (l1[2] + l1[3]));
      const u32 um = __float_as_uint(mx[hh]), ul = __float_as_uint(lt);
      if (g == 0) wst16(R, OFF_ML + (u32)((w * 128 + chunk) * 32 + 16 * hh + n) * 16, (u32x4){um, ul, rs + E, um + ul + E});
    }
    bar();
    TS(8);
  }
  const u32 t8 = E & 0xFFu, t16 = E & 0xFFFFu;
  if (cls == 0) {
    const u32 base = (u32)(h2 * 8 + 2 * cq) * 128;
    const u32 s0i = 16 * w + (l >> 3);
    const u32 oo = OFF_OP + (base + s0i) * 128 + (l & 7) * 16;
    const u32 kq = OFF_ML + ((2 * cq * 128 + s0i) * 32 + h2) * 16;
    u32x4 o[4], k[4];
    vm_wait0();
    bar();
    TS(4);
    u32x4 Fv[8], Fo[16];
    tr8x8(Wv, Fv);
    read_tr(Fo);
#pragma unroll
    for (int i = 0; i < 16; ++i) pin(Fo[i]);
#pragma unroll
    for (int i = 0; i < 8; ++i) pin(Fv[i]);
    TS(5);
    if (OP_TH) {
      if (w == 0) {
        const u32 go = OFF_ML + ((2 * cq * 128 + l) * 32 + h2) * 16;
        for (int it = 0; it < SPIN_MAX; ++it) {
          const u32 z = opq(go);
          const u32x4 g0 = wld16(R, z), g1 = wld16(R, z + 64 * 512);
          const int c = __builtin_popcountll(__ballot(g0[0] + g0[1] + E == g0[3])) + __builtin_popcountll(__ballot(g1[0] + g1[1] + E == g1[3]));
          if (c >= OP_TH) break;
          __builtin_amdgcn_s_sleep(OP_PS);
        }
      }
      bar();
    }
    TS(6);
#pragma unroll
    for (int i = 0; i < 4; ++i) o[i] = wld16(R, oo + (i & 1) * 1024 + (i >> 1) * 16384);
#pragma unroll
    for (int i = 0; i < 4; ++i) k[i] = wld16(R, kq + (i & 1) * 4096 + (i >> 1) * 65536);
    for (int it = 0; it < SPIN_MAX; ++it) {
      bool nb[4];
#pragma unroll
      for (int i = 0; i < 4; ++i) nb[i] = (sum8l(sum4(o[i])) + E != k[i][2]) | (k[i][0] + k[i][1] + E != k[i][3]);
      if (!__any(nb[0] | nb[1] | nb[2] | nb[3])) break;
      const u32 z = opq(0u);
#pragma unroll
      for (int i = 0; i < 4; ++i)
        if (nb[i]) { o[i] = wld16(R, oo + z + (i & 1) * 1024 + (i >> 1) * 16384); k[i] = wld16(R, kq + z + (i & 1) * 4096 + (i >> 1) * 65536); }
    }
    TS(9);
    float Mw = fmaxf(__uint_as_float(k[0][0]), __uint_as_float(k[1][0]));
    Mw = fmaxf(Mw, dpp<0x128>(Mw));
    Mw = x32_max(x16_max(Mw));
    const float w0 = __expf(__uint_as_float(k[0][0]) - Mw), w1 = __expf(__uint_as_float(k[1][0]) - Mw);
    float Lw = w0 * __uint_as_float(k[0][1]) + w1 * __uint_as_float(k[1][1]);
    Lw += dpp<0x128>(Lw);
    Lw = x32_sum(x16_sum(Lw));
    if (l == 0) *(LAS f32x2*)(sm + L_QB + w * 8) = (f32x2){Mw, Lw};
    float a[16];
#pragma unroll
    for (int i = 0; i < 4; ++i) {
      a[2 * i] = w0 * bflo(o[0][i]) + w1 * bflo(o[1][i]);
      a[2 * i + 1] = w0 * bfhi(o[0][i]) + w1 * bfhi(o[1][i]);
      a[8 + 2 * i] = w0 * bflo(o[2][i]) + w1 * bflo(o[3][i]);
      a[8 + 2 * i + 1] = w0 * bfhi(o[2][i]) + w1 * bfhi(o[3][i]);
    }
    float r8[8];
#pragma unroll
    for (int i = 0; i < 8; ++i) {
      auto sw = __builtin_amdgcn_permlane16_swap(__float_as_uint(a[i]), __float_as_uint(a[8 + i]), false, false);
      float t = __uint_as_float(sw[0]) + __uint_as_float(sw[1]);
      t += dpp<0x128>(t);
      r8[i] = x32_sum(t);
    }
    if (l < 32 && n < 8) {
      LAS f32x4* r = (LAS f32x4*)(sm + L_RED + w * 512 + g * 256 + n * 32);
      r[0] = (f32x4){r8[0], r8[1], r8[2], r8[3]}; r[1] = (f32x4){r8[4], r8[5], r8[6], r8[7]};
    }
    TS(11);
    bar();
    TS(12);
    if (w < 4) {
      const LAS f32x4* r = (const LAS f32x4*)(sm + L_RED + (n & 1) * 2048 + (w >> 1) * 256 + (16 * g + 8 * ((n >> 1) & 1)) * 4);
      const f32x4 e0 = *(const LAS f32x4*)(sm + L_QB + (n & 1) * 32), e1 = *(const LAS f32x4*)(sm + L_QB + (n & 1) * 32 + 16);
      const float M = fmaxf(fmaxf(e0[0], e0[2]), fmaxf(e1[0], e1[2]));
      float f0 = __expf(e0[0] - M), f1 = __expf(e0[2] - M), f2 = __expf(e1[0] - M), f3 = __expf(e1[2] - M);
      const float inv = rcp((f0 * e0[1] + f1 * e0[3]) + (f2 * e1[1] + f3 * e1[3]));
      f0 *= inv; f1 *= inv; f2 *= inv; f3 *= inv;
      f32x4 lo = (r[0] * f0 + r[32] * f1) + (r[64] * f2 + r[96] * f3), hi = (r[1] * f0 + r[33] * f1) + (r[65] * f2 + r[97] * f3);
      const u32x4 af = {pk2(lo[0], lo[1]), pk2(lo[2], lo[3]), pk2(hi[0], hi[1]), pk2(hi[2], hi[3])};
      float v0[8], v1[8];
#pragma unroll
      for (int i = 0; i < 8; ++i) {
        f32x4 c = mfma(af, Fv[i], Z4);
        float a0 = kg ? c[2] : c[0], a1 = kg ? c[3] : c[1];
        v0[i] = a0 + dpp<0x128>(a0);
        v1[i] = a1 + dpp<0x128>(a1);
      }
      if (l < 8) {
        LAS char* d = sm + L_HB + (w >> 1) * 512 + (w & 1) * 128 + l * 16;
        *(LAS u32x4*)d = (u32x4){pk2(v0[0], v0[1]), pk2(v0[2], v0[3]), pk2(v0[4], v0[5]), pk2(v0[6], v0[7])};
        *(LAS u32x4*)(d + 256) = (u32x4){pk2(v1[0], v1[1]), pk2(v1[2], v1[3]), pk2(v1[4], v1[5]), pk2(v1[6], v1[7])};
      }
    }
    bar();
    TS(13);
    {
      f32x4 acc[4] = {Z4, Z4, Z4, Z4};
#pragma unroll
      for (int ks = 0; ks < 4; ++ks) {
        u32x4 vf = *(const LAS u32x4*)(sm + L_HB + (n & 3) * 256 + ks * 64 + g * 16);
#pragma unroll
        for (int i = 0; i < 4; ++i) acc[i] = mfma(Fo[4 * ks + i], vf, acc[i]);
      }
      u32x4 ov;
#pragma unroll
      for (int r = 0; r < 4; ++r) {
        const float lo = (n & 4) ? acc[1][r] : acc[0][r], hi = (n & 4) ? acc[3][r] : acc[2][r];
        const float v = (n & 8) ? hi : lo;
        ov[r] = tg8(v + dpp<0x4E>(v), t8);
      }
      if (!(n & 2)) wst16(R, OFF_OUTP + (u32)((n & 1) * 16 + 2 * w + (n >> 3)) * 16384 + chunk * 128 + ((n >> 2) & 1) * 64 + g * 16, ov);
    }
    TS(10);
  } else if (chunk < 32) {
    const int b = chunk >> 4, m = (chunk & 15) * 32 + (l & 31);
    const u32 oo = OFF_OUTP + (u32)chunk * 16384 + w * 2048 + l * 16;
    const u32 go = OFF_MODG + (u32)xcd * 16384 + b * 2048 + m * 4;
    u32 ga = wld4(R, go), gb = wld4(R, go + 12288);
    const float xx = bflo(gld2(x + b * 512 + m));
    for (int it = 0; it < SPIN_MAX; ++it) {
      if (!__any((((ga ^ E) | (gb ^ E)) & 0xFFFFu) != 0u)) break;
      const u32 o = opq(go);
      ga = wld4(R, o); gb = wld4(R, o + 12288);
    }
    u32x4 v0 = wld16(R, oo), v1 = wld16(R, oo + 1024);
    for (int it = 0; it < SPIN_MAX; ++it) {
      const bool n0 = bad8(v0, E), n1 = bad8(v1, E);
      if (!__any(n0 | n1)) break;
      __builtin_amdgcn_s_sleep(1);
      const u32 o = opq(oo);
      if (n0) v0 = wld16(R, o);
      if (n1) v1 = wld16(R, o + 1024);
    }
    TS(11);
    f32x4 s4;
#pragma unroll
    for (int i = 0; i < 4; ++i) { float t = un8(v0[i]) + un8(v1[i]); t += dpp<0x128>(t); s4[i] = x32_sum(x16_sum(t)); }
    if (l < 8) *(LAS f32x4*)(sm + L_RED + w * 128 + l * 16) = s4;
    bar();
    const LAS float* r = (const LAS float*)(sm + L_RED + (l & 31) * 4);
    const float at = rb(((r[0] + r[32]) + (r[64] + r[96])) + ((r[128] + r[160]) + (r[192] + r[224])));
    const float x1 = rb(xx + rb(bfhi(ga) * at));
    if (l < 32) wst4(R, OFF_X1 + (u32)w * 4096 + b * 2048 + m * 4, tg16(x1, t16));
    if (chunk == 0 && tid == 0) wst4(R, 0, E);
    TS(12);
    const u32 fo = OFF_FIN + (u32)chunk * 8192 + w * 896 + (l < 56 ? l : l - 56) * 16;
    u32x4 f = wld16(R, fo);
    for (int it = 0; it < SPIN_MAX; ++it) {
      if (!__any(bad8(f, E))) break;
      __builtin_amdgcn_s_sleep(1);
      f = wld16(R, opq(fo));
    }
    TS(13);
#pragma unroll
    for (int i = 0; i < 4; ++i) { float t = l < 56 ? un8(f[i]) : 0.f; t += dpp<0x128>(t); s4[i] = x32_sum(x16_sum(t)); }
    if (l < 8) *(LAS f32x4*)(sm + L_RED + 1024 + w * 128 + l * 16) = s4;
    bar();
    if (w == 0) {
      const float y = rb(((r[256] + r[288]) + (r[320] + r[352])) + ((r[384] + r[416]) + (r[448] + r[480])));
      const float o = x1 + rb(bfhi(gb) * y);
      if (l < 32) *(GAS u16*)(out + b * 512 + m) = (u16)(pk2(o, o) & 0xFFFFu);
    }
    TS(14);
  } else if (chunk < 64) {
    const u32 pm = mod_cols(chunk - 16);
    const u32 o = OFF_MODG + (u32)w * 16384 + ((chunk - 32) >> 3) * 4096 + ((chunk & 7) * 64 + l) * 4;
    wst4(R, o, (pm << 16) | t16); wst4(R, o + 2048, (pm & 0xFFFF0000u) | t16);
    TS(10);
  } else if (fi >= 0) {
    u32x4 W[8], FG[8];
    {
      const u16* p = (colblk < 4 ? wg : wu) + (size_t)(64 * w + 32 * kg + 8 * g) * 1792 + fi * 32 + 8 * (colblk & 3);
#pragma unroll
      for (int i = 0; i < 8; ++i) W[i] = gld16(p + i * 1792);
    }
    u32x2 Wd[8];
    {
      const u16* p = wd + (size_t)(fi * 32 + 8 * g) * 512 + 64 * w + 4 * n;
#pragma unroll
      for (int i = 0; i < 8; ++i) Wd[i] = gl8<NT_W1>(p + i * 512);
    }
    sched_fence();
    u32x4 Fd[4];
    tr8x8(W, FG);
    tr8x4(Wd, Fd);
#pragma unroll
    for (int i = 0; i < 8; ++i) pin(FG[i]);
#pragma unroll
    for (int i = 0; i < 4; ++i) pin(Fd[i]);
    TS(10);
    if (w < 2) {
      const u32 so = OFF_MODG + (u32)xcd * 16384 + 4096 + (u32)w * 2048 + l * 16;
      u32x4 sb0 = wld16(R, so), sb1 = wld16(R, so + 1024), cb0 = wld16(R, so + 4096), cb1 = wld16(R, so + 5120);
      for (int it = 0; it < SPIN_MAX; ++it) {
        if (!__any(bad16(sb0, E) | bad16(sb1, E) | bad16(cb0, E) | bad16(cb1, E))) break;
        const u32 o = opq(so);
        sb0 = wld16(R, o); sb1 = wld16(R, o + 1024); cb0 = wld16(R, o + 4096); cb1 = wld16(R, o + 5120);
      }
      float c1[8], sh[8];
#pragma unroll
      for (int i = 0; i < 4; ++i) {
        c1[i] = rb(1.f + bfhi(cb0[i])); c1[4 + i] = rb(1.f + bfhi(cb1[i]));
        sh[i] = bfhi(sb0[i]); sh[4 + i] = bfhi(sb1[i]);
      }
#pragma unroll
      for (int i = 0; i < 8; ++i) { pin(c1[i]); pin(sh[i]); }
      TS(11);
      const u32 xo = OFF_X1 + (u32)xcd * 4096 + (u32)w * 2048 + l * 16;
      u32x4 a0 = wld16(R, xo), a1 = wld16(R, xo + 1024);
      for (int it = 0; it < SPIN_MAX; ++it) {
        if (!__any(bad16(a0, E) | bad16(a1, E))) break;
        const u32 o = opq(xo);
        a0 = wld16(R, o); a1 = wld16(R, o + 1024);
      }
      TS(12);
      float xv[8];
#pragma unroll
      for (int i = 0; i < 4; ++i) { xv[i] = bfhi(a0[i]); xv[4 + i] = bfhi(a1[i]); }
      const float m = wave_sum(((xv[0] + xv[1]) + (xv[2] + xv[3])) + ((xv[4] + xv[5]) + (xv[6] + xv[7]))) * (1.f / 512.f);
      float qq = 0.f;
#pragma unroll
      for (int i = 0; i < 8; ++i) { float d = xv[i] - m; qq += d * d; }
      const float var = wave_sum(qq) * (1.f / 512.f);
      const float mean = rb(m), sd = rcp(rb(__builtin_amdgcn_sqrtf(rb(rb(var) + 1e-5f))));
      float hv[8];
#pragma unroll
      for (int i = 0; i < 8; ++i) hv[i] = rb(rb(rb(xv[i] - mean) * sd) * c1[i]) + sh[i];
      *(LAS u32x2*)(sm + L_HB + w * 1024 + l * 8) = (u32x2){pk2(hv[0], hv[1]), pk2(hv[2], hv[3])};
      *(LAS u32x2*)(sm + L_HB + w * 1024 + 512 + l * 8) = (u32x2){pk2(hv[4], hv[5]), pk2(hv[6], hv[7])};
    }
    bar();
    {
      u32x4 af = *(const LAS u32x4*)(sm + L_HB + (n & 1) * 1024 + (64 * w + 32 * ((n >> 1) & 1) + 8 * g) * 2);
      float v0[8], v1[8];
#pragma unroll
      for (int i = 0; i < 8; ++i) {
        f32x4 a = mfma(af, FG[i], Z4);
        float a0 = kg ? a[2] : a[0], a1 = kg ? a[3] : a[1];
        v0[i] = a0 + dpp<0x128>(a0);
        v1[i] = a1 + dpp<0x128>(a1);
      }
      if (l < 8) {
        LAS f32x4* r = (LAS f32x4*)(sm + L_RED + 4096 + w * 512 + l * 32);
        r[0] = (f32x4){v0[0], v0[1], v0[2], v0[3]}; r[1] = (f32x4){v0[4], v0[5], v0[6], v0[7]};
        r[16] = (f32x4){v1[0], v1[1], v1[2], v1[3]}; r[17] = (f32x4){v1[4], v1[5], v1[6], v1[7]};
      }
    }
    bar();
    {
      const LAS float* r = (const LAS float*)(sm + L_RED + 4096 + (l >> 5) * 256 + (l & 31) * 4);
      float gs = ((r[0] + r[128]) + (r[256] + r[384])) + ((r[512] + r[640]) + (r[768] + r[896]));
      float us = ((r[32] + r[160]) + (r[288] + r[416])) + ((r[544] + r[672]) + (r[800] + r[928]));
      float hv = rb(gelu_f(rb(gs))) * rb(us);
      *(LAS u16*)(sm + L_P + w * 128 + l * 2) = (u16)(pk2(hv, hv) & 0xFFFFu);
      lds_wait();
      u32x4 ha = *(const LAS u32x4*)(sm + L_P + w * 128 + (n & 1) * 64 + g * 16);
      u32x4 o;
#pragma unroll
      for (int i = 0; i < 4; ++i) {
        f32x4 a = mfma(ha, Fd[i], Z4);
        o[i] = tg8((g & 1) ? a[1] : a[0], t8);
      }
      if (g < 2) wst16(R, OFF_FIN + (u32)(g * 16 + 2 * w + (n >> 3)) * 8192 + fi * 128 + (n & 7) * 16, o);
    }
    TS(13);
  }
  if (PF_CODE) asm volatile(".Lwc_b:");
}

extern "C" void kernel_launch(void* const* d_in, const int* in_sizes, int n_in, void* d_out, int out_size, void* d_ws,
                              size_t ws_size, hipStream_t stream) {
  (void)in_sizes; (void)n_in; (void)out_size; (void)ws_size;
  (void)hipFuncSetAttribute((const void*)fused_kernel, hipFuncAttributeMaxDynamicSharedMemorySize, (int)LDS_BYTES);
  hipLaunchKernelGGL(fused_kernel, dim3(256), dim3(512), LDS_BYTES, stream, (const u16*)d_in[0], (const u16*)d_in[1],
                     (const u16*)d_in[2], (const u16*)d_in[3], (const u16*)d_in[4], (const u16*)d_in[5], (const u16*)d_in[7],
                     (const u16*)d_in[8], (const u16*)d_in[9], (const u16*)d_in[10], (const u16*)d_in[11], (u16*)d_out, (char*)d_ws);
}
```

```cpp
#include <hip/hip_runtime.h>
#include <hip/hip_bf16.h>
#include <stdint.h>
#include <type_traits>

typedef unsigned short u16;
typedef unsigned int u32;
typedef __attribute__((ext_vector_type(4))) u32 u32x4;
typedef __attribute__((ext_vector_type(2))) u32 u32x2;
typedef __attribute__((ext_vector_type(4))) float f32x4;
typedef __attribute__((ext_vector_type(2))) float f32x2;
typedef __attribute__((ext_vector_type(8))) __bf16 bf16x8;
typedef __attribute__((ext_vector_type(2))) __bf16 bf16x2;
typedef __attribute__((ext_vector_type(4))) short s16x4;
#define LAS __attribute__((address_space(3)))
#define GAS __attribute__((address_space(1)))
#define DI __device__ __forceinline__

constexpr u32 OFF_HINT = 4096;
constexpr u32 OFF_HM = OFF_HINT + 16384;
constexpr u32 OFF_MODG = OFF_HM + 65536;
constexpr u32 OFF_X1 = OFF_MODG + 131072;
constexpr u32 OFF_QA = OFF_X1 + 32768;
constexpr u32 OFF_QK = OFF_QA + 131072;
constexpr u32 OFF_OP = OFF_QK + 65536;
constexpr u32 OFF_ML = OFF_OP + 4194304;
constexpr u32 OFF_OUTP = OFF_ML + 524288;
constexpr u32 OFF_FIN = OFF_OUTP + 524288;
constexpr u32 WS_BYTES = OFF_FIN + 262144;
constexpr u32 OFF_DBG = 8u << 20;
static_assert(WS_BYTES <= OFF_DBG, "ws");
constexpr int SPIN_MAX = 1 << 16;

constexpr u32 L_C = 0;
constexpr u32 L_Q = 0;
constexpr u32 L_P = 32768;
constexpr u32 L_MX = L_P + 8704;
constexpr u32 L_LS = L_MX + 1024;
constexpr u32 L_RED = 131072;
constexpr u32 L_HB = L_RED + 8192;
constexpr u32 L_QB = L_HB + 2048;
constexpr u32 L_DUM = L_QB + 512;
constexpr u32 LDS_BYTES = L_DUM + 256;
static_assert(LDS_BYTES <= 163840, "lds");

DI float bflo(u32 v) { return __builtin_bit_cast(float, v << 16); }
DI float bfhi(u32 v) { return __builtin_bit_cast(float, v & 0xFFFF0000u); }
DI u32 pk2(float a, float b) {
  f32x2 f = {a, b};
  return __builtin_bit_cast(u32, __builtin_convertvector(f, bf16x2));
}
DI float rb(float a) { return bflo(pk2(a, a)); }
DI f32x2 rb2(f32x2 v) { const u32 p = pk2(v[0], v[1]); return (f32x2){bflo(p), bfhi(p)}; }
DI float mx2(float a, float b) { float r; asm("v_max_f32 %0, %1, %2" : "=v"(r) : "v"(a), "v"(b)); return r; }
DI float mx3(float a, float b, float c) { float r; asm("v_max3_f32 %0, %1, %2, %3" : "=v"(r) : "v"(a), "v"(b), "v"(c)); return r; }
DI f32x4 mfma(u32x4 a, u32x4 b, f32x4 c) {
  return __builtin_amdgcn_mfma_f32_16x16x32_bf16(__builtin_bit_cast(bf16x8, a), __builtin_bit_cast(bf16x8, b), c, 0, 0, 0);
}
template <int CTRL> DI float dpp(float x) {
  return __builtin_bit_cast(float, __builtin_amdgcn_update_dpp(0, __builtin_bit_cast(int, x), CTRL, 0xF, 0xF, true));
}
DI float x16_sum(float x) { auto s = __builtin_amdgcn_permlane16_swap(__float_as_uint(x), __float_as_uint(x), false, false); return __uint_as_float(s[0]) + __uint_as_float(s[1]); }
DI float x32_sum(float x) { auto s = __builtin_amdgcn_permlane32_swap(__float_as_uint(x), __float_as_uint(x), false, false); return __uint_as_float(s[0]) + __uint_as_float(s[1]); }
DI float x16_max(float x) { auto s = __builtin_amdgcn_permlane16_swap(__float_as_uint(x), __float_as_uint(x), false, false); return mx2(__uint_as_float(s[0]), __uint_as_float(s[1])); }
DI float x32_max(float x) { auto s = __builtin_amdgcn_permlane32_swap(__float_as_uint(x), __float_as_uint(x), false, false); return mx2(__uint_as_float(s[0]), __uint_as_float(s[1])); }
DI float wave_sum(float x) {
  x += dpp<0xB1>(x); x += dpp<0x4E>(x); x += dpp<0x141>(x); x += dpp<0x140>(x);
  return x32_sum(x16_sum(x));
}
DI float wave_max(float x) {
  x = mx2(x, dpp<0xB1>(x)); x = mx2(x, dpp<0x4E>(x)); x = mx2(x, dpp<0x141>(x)); x = mx2(x, dpp<0x140>(x));
  return x32_max(x16_max(x));
}
DI float silu_f(float x) { return x * __builtin_amdgcn_rcpf(1.f + __expf(-x)); }
DI float gelu_f(float g) {
  float z = 0.7978845608028654f * (g + 0.044715f * g * g * g);
  float e = __expf(2.f * z);
  float t = 1.f - 2.f * __builtin_amdgcn_rcpf(e + 1.f);
  return 0.5f * g * (1.f + t);
}
DI u32x4 gld16(const void* p) { return *(const GAS u32x4*)p; }
DI u32x2 gld8(const void* p) { return *(const GAS u32x2*)p; }
DI u32 gld2(const void* p) { return *(const GAS u16*)p; }
typedef __amdgpu_buffer_rsrc_t rsrc_t;
DI u32x4 wld16(rsrc_t r, u32 off) { return __builtin_amdgcn_raw_buffer_load_b128(r, off, 0, 16); }
DI u32x2 wld8(rsrc_t r, u32 off) { return __builtin_amdgcn_raw_buffer_load_b64(r, off, 0, 16); }
DI u32 wld4(rsrc_t r, u32 off) { return __builtin_amdgcn_raw_buffer_load_b32(r, off, 0, 16); }
DI void wst16(rsrc_t r, u32 off, u32x4 v) { __builtin_amdgcn_raw_buffer_store_b128(v, r, off, 0, 16); }
DI void wst8(rsrc_t r, u32 off, u32x2 v) { __builtin_amdgcn_raw_buffer_store_b64(v, r, off, 0, 16); }
DI void wst4(rsrc_t r, u32 off, u32 v) { __builtin_amdgcn_raw_buffer_store_b32(v, r, off, 0, 16); }
DI u32 opq(u32 v) { asm volatile("" : "+v"(v)); return v; }
template <class T> DI void pin(T& v) { asm volatile("" : "+v"(v)); }
DI float rcp(float x) { return __builtin_amdgcn_rcpf(x); }
DI void fence_c() { asm volatile("" ::: "memory"); }
DI void sched_fence() { __builtin_amdgcn_sched_barrier(0); }
DI void lds_wait() { asm volatile("s_waitcnt lgkmcnt(0)" ::: "memory"); }
DI void bar() { asm volatile("s_waitcnt lgkmcnt(0)\n\ts_barrier" ::: "memory"); }
DI void tr8x8(const u32x4 (&W)[8], u32x4 (&F)[8]) {
#pragma unroll
  for (int i = 0; i < 8; ++i)
#pragma unroll
    for (int p = 0; p < 4; ++p)
      F[i][p] = __builtin_amdgcn_perm(W[2 * p + 1][i >> 1], W[2 * p][i >> 1], (i & 1) ? 0x07060302u : 0x05040100u);
}
DI void tr8x4(const u32x2* W, u32x4* F) {
#pragma unroll
  for (int i = 0; i < 4; ++i)
#pragma unroll
    for (int p = 0; p < 4; ++p)
      F[i][p] = __builtin_amdgcn_perm(W[2 * p + 1][i >> 1], W[2 * p][i >> 1], (i & 1) ? 0x07060302u : 0x05040100u);
}
DI void ln_stats(u32x4 v, float& mean, float& sd) {
  float s = 0.f;
#pragma unroll
  for (int i = 0; i < 4; ++i) s += bflo(v[i]) + bfhi(v[i]);
  float m = wave_sum(s) * (1.f / 512.f);
  float q = 0.f;
#pragma unroll
  for (int i = 0; i < 4; ++i) { float a = bflo(v[i]) - m, b = bfhi(v[i]) - m; q += a * a + b * b; }
  float var = wave_sum(q) * (1.f / 512.f);
  mean = rb(m);
  sd = __builtin_amdgcn_rcpf(rb(__builtin_amdgcn_sqrtf(rb(rb(var) + 1e-5f))));
}
DI float ln_mod(float x, float mean, float sd, float sc, float sh) {
  float ln = rb(rb(x - mean) * sd);
  return rb(rb(ln * rb(1.f + sc)) + sh);
}

#define PF_TLB 1
#define PF_CODE 1
#define PF_LATE 0
#define PF_C 0
#define PF_NW 6
#define NT_WQ 1
#define NT_CB 1
#define NT_CA 0
#define NT_W1 1
#define HINT_D 170
#define B_DELAY 85
#define OP_SLEEP 0
#define B_GATE 0
#define A_EARLY 0
#define ROT 0
#define OP_TH 12
#define OP_PS 4
#define WO_EARLY 1
#define NT_WO 1
template <int NT> DI u32x4 gl16(const void* p) {
  if (NT) return __builtin_nontemporal_load((const GAS u32x4*)p);
  return *(const GAS u32x4*)p;
}
template <int NT> DI u32x2 gl8(const void* p) {
  if (NT) return __builtin_nontemporal_load((const GAS u32x2*)p);
  return *(const GAS u32x2*)p;
}
template <int NT> DI void dma16(const void* gp, u32 lds) {
  u32 keep;
  if (NT) asm volatile("s_mov_b32 %0, m0\n\ts_mov_b32 m0, %2\n\ts_nop 0\n\tglobal_load_lds_dwordx4 %1, off nt\n\ts_mov_b32 m0, %0" : "=&s"(keep) : "v"(gp), "s"(lds) : "memory");
  else asm volatile("s_mov_b32 %0, m0\n\ts_mov_b32 m0, %2\n\ts_nop 0\n\tglobal_load_lds_dwordx4 %1, off\n\ts_mov_b32 m0, %0" : "=&s"(keep) : "v"(gp), "s"(lds) : "memory");
}
template <int SC> DI void touch4(const void* gp, u32 lds) {
  u32 keep;
  if (SC) asm volatile("s_mov_b32 %0, m0\n\ts_mov_b32 m0, %2\n\ts_nop 0\n\tglobal_load_lds_dword %1, off sc1\n\ts_mov_b32 m0, %0" : "=&s"(keep) : "v"(gp), "s"(lds) : "memory");
  else asm volatile("s_mov_b32 %0, m0\n\ts_mov_b32 m0, %2\n\ts_nop 0\n\tglobal_load_lds_dword %1, off\n\ts_mov_b32 m0, %0" : "=&s"(keep) : "v"(gp), "s"(lds) : "memory");
}
template <int CTRL> DI u32 dppu(u32 x) { return (u32)__builtin_amdgcn_update_dpp(0, (int)x, CTRL, 0xF, 0xF, true); }
DI u32 sum8l(u32 x) { x += dppu<0xB1>(x); x += dppu<0x4E>(x); x += dppu<0x141>(x); return x; }
DI u32 sum16l(u32 x) { x = sum8l(x); x += dppu<0x140>(x); return x; }
DI bool bad16x2(u32x2 v, u32 t) { return (((v[0] ^ t) | (v[1] ^ t)) & 0xFFFFu) != 0u; }
DI void vm_wait0() { asm volatile("s_waitcnt vmcnt(0)" ::: "memory"); }
DI u32 sum4(u32x4 v) { return (v[0] + v[1]) + (v[2] + v[3]); }
DI bool bad16(u32x4 v, u32 t) { return (((v[0] ^ t) | (v[1] ^ t) | (v[2] ^ t) | (v[3] ^ t)) & 0xFFFFu) != 0u; }
DI bool bad8(u32x4 v, u32 t) { return (((v[0] ^ t) | (v[1] ^ t) | (v[2] ^ t) | (v[3] ^ t)) & 0xFFu) != 0u; }
DI u32 tg16(float bfval, u32 t16) { return (__float_as_uint(bfval) & 0xFFFF0000u) | t16; }
DI u32 tg8(float v, u32 t8) { return (__float_as_uint(v) & 0xFFFFFF00u) | t8; }
DI float un8(u32 v) { return __uint_as_float(v & 0xFFFFFF00u); }

#define TS(i) do {} while (0)

__global__ void __launch_bounds__(512, 2)
fused_kernel(const u16* __restrict__ x, const u16* __restrict__ ckv, const u16* __restrict__ wq, const u16* __restrict__ wuk,
             const u16* __restrict__ wuv, const u16* __restrict__ wo, const u16* __restrict__ wg, const u16* __restrict__ wu,
             const u16* __restrict__ wd, const u16* __restrict__ cnd, const u16* __restrict__ wmod, u16* __restrict__ out,
             char* ws) {
  extern __shared__ __attribute__((aligned(16))) char smem_raw[];
  LAS char* sm = (LAS char*)smem_raw;
  const u32 lds0 = (u32)(size_t)sm;
  const int tid = threadIdx.x, l = tid & 63;
  const int w = __builtin_amdgcn_readfirstlane(tid >> 6);
  const int n = l & 15, g = l >> 4;
  const int bid = blockIdx.x, xcd = bid & 7, j = bid >> 3;
  const int cls = (j >= 2 && j < 18) ? 0 : 1, pairidx = j < 2 ? j : (j < 18 ? j - 2 : j - 16);
  const int chunk = xcd + 8 * pairidx;
  const int h2 = 4 * xcd + (pairidx >> 2), cq = pairidx & 3;
  const int fi = chunk - 72;
  const rsrc_t R = __builtin_amdgcn_make_buffer_rsrc(ws, 0, WS_BYTES, 0x00020000);
  const f32x4 Z4 = {0.f, 0.f, 0.f, 0.f};
  TS(0);
  const u32 Ev = *(const GAS u32*)ws + 1u;
  const int colblk = n & 7, kg = n >> 3;
  u32x4 Wv[8];
  const int rot = (pairidx + 2 * w) & 15;
  auto load_rows = [&](const u16* src, int r0, auto nr, auto nt, int ib = 0) {
#pragma unroll
    for (int i = 0; i < decltype(nr)::value; ++i) {
      if (ROT && decltype(nr)::value == 16) {
        const int ii = (i + rot) & 15;
        const u32 swz = ((ii & 3) << 2) | (ii >> 2);
        dma16<decltype(nt)::value>(src + (size_t)(r0 + ii) * 512 + (l ^ swz) * 8, lds0 + L_C + (r0 + ii) * 1024);
      } else {
        const u32 swz = (((i + ib) & 3) << 2) | (((i + ib) >> 2) & 3);
        dma16<decltype(nt)::value>(src + (size_t)(r0 + i) * 512 + (l ^ swz) * 8, lds0 + L_C + (r0 + i) * 1024);
      }
    }
  };
  auto read_tr = [&](u32x4 (&f)[16]) {
    const int q = n >> 2, pp = n & 3;
#pragma unroll
    for (int ks = 0; ks < 4; ++ks)
#pragma unroll
      for (int t = 0; t < 4; ++t)
#pragma unroll
        for (int tt = 0; tt < 2; ++tt) {
          const int row = 32 * ks + 8 * g + 4 * tt + q;
          const u32 swz = (q << 2) | ((2 * g + tt) & 3);
          const u32 ad = L_C + row * 1024 + (((8 * w + 2 * t + (pp >> 1)) ^ swz) * 16) + 8 * (pp & 1);
          u32x2 v = __builtin_bit_cast(u32x2, __builtin_amdgcn_ds_read_tr16_b64_v4i16((LAS s16x4*)(sm + ad)));
          f[4 * ks + t][2 * tt] = v[0]; f[4 * ks + t][2 * tt + 1] = v[1];
        }
  };
  auto mod_cols = [&](int cb) -> u32 {
    u32x4 cfr = gld16(cnd + (n & 1) * 512 + 64 * w + 32 * ((n >> 1) & 1) + 8 * g);
    u32x4 W[8];
    const u16* p = wmod + (size_t)(64 * w + 32 * kg + 8 * g) * 3072 + cb * 64 + 8 * colblk;
#pragma unroll
    for (int i = 0; i < 8; ++i) W[i] = gl16<NT_W1>(p + i * 3072);
    sched_fence();
    u32x4 af;
#pragma unroll
    for (int i = 0; i < 4; ++i) af[i] = pk2(silu_f(bflo(cfr[i])), silu_f(bfhi(cfr[i])));
    pin(af);
    u32x4 F[8];
    tr8x8(W, F);
    float v0[8], v1[8];
#pragma unroll
    for (int i = 0; i < 8; ++i) {
      f32x4 a = mfma(af, F[i], Z4);
      v0[i] = a[0] + dpp<0x128>(a[2]);
      v1[i] = a[1] + dpp<0x128>(a[3]);
    }
    if (l < 8) {
      LAS f32x4* r = (LAS f32x4*)(sm + L_RED + w * 512 + l * 32);
      r[0] = (f32x4){v0[0], v0[1], v0[2], v0[3]}; r[1] = (f32x4){v0[4], v0[5], v0[6], v0[7]};
      r[16] = (f32x4){v1[0], v1[1], v1[2], v1[3]}; r[17] = (f32x4){v1[4], v1[5], v1[6], v1[7]};
    }
    bar();
    const LAS float* r = (const LAS float*)(sm + L_RED + l * 4);
    float m0 = ((r[0] + r[128]) + (r[256] + r[384])) + ((r[512] + r[640]) + (r[768] + r[896]));
    float m1 = ((r[64] + r[192]) + (r[320] + r[448])) + ((r[576] + r[704]) + (r[832] + r[960]));
    return pk2(m0, m1);
  };
  typedef std::integral_constant<int, 16> I16;
  typedef std::integral_constant<int, 32> I32;
  const u16* crow = ckv + (size_t)chunk * 128 * 512;
  const u16* worow = wo + (size_t)h2 * 128 * 512;
  u32x4 cf[16], ca[16];
  auto read_frags = [&]() {
    const u32 swz = ((n & 3) << 2) | (n >> 2);
#pragma unroll
    for (int ks = 0; ks < 16; ++ks) cf[ks] = *(const LAS u32x4*)(sm + L_C + (16 * w + n) * 1024 + (((4 * ks + g) ^ swz) * 16));
    read_tr(ca);
#pragma unroll
    for (int ks = 0; ks < 16; ++ks) { pin(cf[ks]); pin(ca[ks]); }
  };
  auto load_s4 = [&]() {
    if (w < 4) {
      const u16* p = wuv + (size_t)(cq * 128 + (w >> 1) * 64 + 32 * kg + 4 * g) * 4096 + h2 * 128 + 64 * (w & 1) + 8 * colblk;
#pragma unroll
      for (int i = 0; i < 8; ++i) Wv[i] = gl16<NT_W1>(p + (size_t)(16 * (i >> 2) + (i & 3)) * 4096);
    }
    load_rows(worow, 16 * w, I16(), std::integral_constant<int, NT_WO>());
  };
  const u32 ho_ = OFF_HINT + (u32)xcd * 2048 + n * 128;
  auto hint_wait = [&](u32 e, u32x2 hv) {
    if (w == 0) {
      for (int it = 0; it < SPIN_MAX; ++it) {
        if (!__any(hv[0] != e)) break;
        __builtin_amdgcn_s_sleep(2);
        hv = wld8(R, opq(ho_));
      }
      for (int it = 0; it < 48; ++it) {
        if (!__any((u32)__builtin_amdgcn_s_memrealtime() - hv[1] < (u32)HINT_D)) break;
        __builtin_amdgcn_s_sleep(1);
      }
    }
    bar();
  };
  auto warm_pages = [&]() {
    if (PF_TLB && w == 7) {
      const char* pa = ws + min((u32)l * 131072u, WS_BYTES - 128u);
      if (l == 63) pa = (const char*)out;
      if (l == 62) pa = (const char*)(wuv + (size_t)cq * 128 * 4096);
      if (l == 61) pa = (const char*)(wuv + (size_t)(cq * 128 + 127) * 4096);
      if (l == 60) pa = (const char*)worow;
      if (l == 59) pa = (const char*)wg;
      if (l == 58) pa = (const char*)(wg + 512 * 1792 - 64);
      if (l == 57) pa = (const char*)wu;
      if (l == 56) pa = (const char*)(wu + 512 * 1792 - 64);
      if (l == 55) pa = (const char*)wd;
      if (l == 54) pa = (const char*)(wd + 512 * 1792 - 64);
      if (l == 53) pa = (const char*)x;
      touch4<1>(pa, lds0 + L_DUM);
    }
  };
  auto warm_c = [&]() {
    if (PF_C && w == 6) touch4<1>((l & 4) ? ckv + (size_t)(xcd + 8 * (l & 1)) * 65536 : crow + (l & 3) * 16384, lds0 + L_DUM);
  };
  auto warm_code = [&]() {
    if (PF_CODE) {
      unsigned long long pc; u32 len;
      asm volatile("s_getpc_b64 %0\n.Lwc_a:\n\ts_mov_b32 %1, .Lwc_b-.Lwc_a" : "=s"(pc), "=s"(len));
      touch4<0>((const char*)pc + min((u32)tid * 128u, len - 4u), lds0 + L_DUM);
    }
  };
  const u32 rb_ = (u32)(xcd & 1) * 2 + (chunk >> 6);
  u32 E;

  if (cls == 0) {
    u32x4 xv0 = gld16(x + 8 * l), xv1 = gld16(x + 512 + 8 * l);
    const u32 xm0 = *(const GAS u32*)(x + 2 * (tid & 255)), xm1 = *(const GAS u32*)(x + 512 + 2 * (tid & 255));
    u32x4 W[16];
    {
      const u16* p = wq + (size_t)(64 * w + 8 * g) * 4096 + h2 * 128 + 8 * n;
#pragma unroll
      for (int i = 0; i < 16; ++i) W[i] = gl16<NT_WQ>(p + (size_t)(32 * (i >> 3) + (i & 7)) * 4096);
    }
#pragma unroll
    for (int i = 0; i < 4; ++i)
      dma16<NT_W1>(wuk + (size_t)(128 * cq + 16 * w + 4 * i + g) * 4096 + h2 * 128 + (n ^ (4 * i + g)) * 8, lds0 + L_C + w * 16384 + 12288 + i * 1024);
    if (!PF_LATE) warm_pages();
    sched_fence();
    E = __builtin_amdgcn_readfirstlane(Ev);
    float mean0, sd0, mean1, sd1;
    ln_stats(xv0, mean0, sd0);
    ln_stats(xv1, mean1, sd1);
    float ln00 = rb(rb(bflo(xm0) - mean0) * sd0), ln01 = rb(rb(bflo(xm1) - mean1) * sd1);
    float ln10 = rb(rb(bfhi(xm0) - mean0) * sd0), ln11 = rb(rb(bfhi(xm1) - mean1) * sd1);
    u32x4 F[16];
    tr8x8(*(const u32x4(*)[8])(W), *(u32x4(*)[8])(F));
    tr8x8(*(const u32x4(*)[8])(W + 8), *(u32x4(*)[8])(F + 8));
#pragma unroll
    for (int i = 0; i < 16; ++i) pin(F[i]);
    pin(ln00); pin(ln01); pin(ln10); pin(ln11);
    vm_wait0();
    u32x4 uk[4];
#pragma unroll
    for (int ks = 0; ks < 4; ++ks) uk[ks] = *(const LAS u32x4*)(sm + L_C + w * 16384 + 12288 + n * 256 + (((4 * ks + g) ^ n) * 16));
#pragma unroll
    for (int i = 0; i < 4; ++i) pin(uk[i]);
    TS(1);
    if (w < 4) {
      const u32 ho = OFF_HM + (u32)xcd * 8192 + tid * 16;
      u32x4 hs = wld16(R, ho), hc = wld16(R, ho + 4096);
      for (int it = 0; it < SPIN_MAX; ++it) {
        if (!__any(bad16(hs, E) | bad16(hc, E))) break;
        const u32 o = opq(ho);
        hs = wld16(R, o); hc = wld16(R, o + 4096);
      }
      const float h00 = rb(ln00 * rb(1.f + bfhi(hc[0]))) + bfhi(hs[0]), h01 = rb(ln01 * rb(1.f + bfhi(hc[1]))) + bfhi(hs[1]);
      const float h10 = rb(ln10 * rb(1.f + bfhi(hc[2]))) + bfhi(hs[2]), h11 = rb(ln11 * rb(1.f + bfhi(hc[3]))) + bfhi(hs[3]);
      *(LAS u32*)(sm + L_HB + tid * 4) = pk2(h00, h10);
      *(LAS u32*)(sm + L_HB + 1024 + tid * 4) = pk2(h01, h11);
    }
    TS(2);
    bar();
    {
      u32x4 a0 = *(const LAS u32x4*)(sm + L_HB + (n & 1) * 1024 + w * 128 + g * 16);
      u32x4 a1 = *(const LAS u32x4*)(sm + L_HB + (n & 1) * 1024 + w * 128 + 64 + g * 16);
      f32x4 acc[8];
#pragma unroll
      for (int i = 0; i < 8; ++i) acc[i] = mfma(a0, F[i], Z4);
#pragma unroll
      for (int i = 0; i < 8; ++i) acc[i] = mfma(a1, F[8 + i], acc[i]);
      if (g == 0) {
        LAS f32x4* r = (LAS f32x4*)(sm + L_RED + w * 1024 + n * 32);
        r[0] = (f32x4){acc[0][0], acc[1][0], acc[2][0], acc[3][0]}; r[1] = (f32x4){acc[4][0], acc[5][0], acc[6][0], acc[7][0]};
        r[32] = (f32x4){acc[0][1], acc[1][1], acc[2][1], acc[3][1]}; r[33] = (f32x4){acc[4][1], acc[5][1], acc[6][1], acc[7][1]};
      }
    }
    bar();
    if (w < 4) {
      const LAS float* r = (const LAS float*)(sm + L_RED + tid * 4);
      float s = ((r[0] + r[256]) + (r[512] + r[768])) + ((r[1024] + r[1280]) + (r[1536] + r[1792]));
      *(LAS u16*)(sm + L_QB + tid * 2) = (u16)(pk2(s, s) & 0xFFFFu);
    }
    bar();
    {
      u32x4 qb[4];
#pragma unroll
      for (int ks = 0; ks < 4; ++ks) qb[ks] = *(const LAS u32x4*)(sm + L_QB + (n & 1) * 256 + ks * 64 + g * 16);
      f32x4 d0 = mfma(uk[0], qb[0], Z4), d1 = mfma(uk[1], qb[1], Z4);
      d0 = mfma(uk[2], qb[2], d0); d1 = mfma(uk[3], qb[3], d1);
      d0 += d1;
      if (n < 2) *(LAS u32x2*)(sm + L_HB + n * 256 + w * 32 + g * 8) = (u32x2){pk2(d0[0], d0[1]), pk2(d0[2], d0[3])};
    }
    bar();
    if (w == 0) {
      const u32x4 pay = *(const LAS u32x4*)(sm + L_HB + (g & 1) * 256 + n * 16);
      wst16(R, OFF_QA + (u32)(g * 32 + h2) * 1024 + cq * 256 + n * 16, pay);
      const u32 ck = sum16l(sum4(pay)) + E;
      if (n < 8) wst16(R, OFF_QK + (u32)((g * 32 + h2) * 4 + cq) * 128 + n * 16, (u32x4){ck, ck, ck, ck});
    }
    TS(3);
    if (PF_LATE) warm_pages();
    load_s4();
  } else {
    if (chunk < 16) {
      const u32 pm = mod_cols(chunk);
      TS(1);
      E = __builtin_amdgcn_readfirstlane(Ev);
      const u32 tt16 = E & 0xFFFFu;
      wst8(R, OFF_HM + (u32)w * 8192 + (chunk >> 3) * 4096 + ((chunk & 7) * 64 + l) * 8, (u32x2){(pm << 16) | tt16, (pm & 0xFFFF0000u) | tt16});
      const u32 T = (u32)__builtin_amdgcn_s_memrealtime();
      if (l < 8) wst16(R, OFF_HINT + (u32)w * 2048 + chunk * 128 + l * 16, (u32x4){E, T, E, T});
      TS(2);
      warm_pages();
    } else {
      if (pairidx < PF_NW) warm_code();
      warm_c();
      if (!PF_LATE) warm_pages();
      if (B_DELAY) __builtin_amdgcn_s_sleep(B_DELAY);
      if (PF_LATE) warm_pages();
    }
    load_rows(crow, 16 * w, I16(), std::integral_constant<int, NT_CB>());
    E = __builtin_amdgcn_readfirstlane(Ev);
    vm_wait0();
    const u32x2 hv0 = wld8(R, ho_);
    bar();
    TS(3);
    read_frags();
    TS(4);
    u32 qad[4];
#pragma unroll
    for (int b4 = 0; b4 < 4; ++b4) { qad[b4] = lds0 + L_Q + n * 1024 + (((4 * b4 + g) ^ (((n & 3) << 2) | (n >> 2))) * 16); pin(qad[b4]); }
    hint_wait(E, hv0);
    {
      const u32 qo = OFF_QA + (rb_ * 32 + 4 * w) * 1024 + l * 16, ko = OFF_QK + ((rb_ * 32 + 4 * w + (n & 3)) * 4 + g) * 128;
      u32x4 q[4];
#pragma unroll
      for (int i = 0; i < 4; ++i) q[i] = wld16(R, qo + i * 1024);
      u32 qk = wld4(R, ko);
      for (int it = 0; it < SPIN_MAX; ++it) {
        const u32 c0 = sum16l(sum4(q[0])), c1 = sum16l(sum4(q[1])), c2 = sum16l(sum4(q[2])), c3 = sum16l(sum4(q[3]));
        const u32 cs = (n & 2) ? ((n & 1) ? c3 : c2) : ((n & 1) ? c1 : c0);
        const unsigned long long bad = __ballot(cs + E != qk);
        if (!bad) break;
        const u32 z = opq(0u);
#pragma unroll
        for (int i = 0; i < 4; ++i)
          if (bad & (0x1111111111111111ull << i)) q[i] = wld16(R, qo + z + i * 1024);
        qk = wld4(R, ko + z);
      }
#pragma unroll
      for (int i = 0; i < 4; ++i) *(LAS u32x4*)(sm + L_Q + (4 * w + i) * 1024 + (l ^ ((i << 2) | (w & 3))) * 16) = q[i];
    }
    TS(5);
    sched_fence();
    bar();
    TS(6);
    f32x4 sa[2];
    {
      u32x4 qf[2][8];
#pragma unroll
      for (int u = 0; u < 2; ++u)
#pragma unroll
        for (int ks = 0; ks < 8; ++ks) qf[u][ks] = *(const LAS u32x4*)(size_t)(qad[ks & 3] + (2 * u + (ks >> 2)) * 256);
      sched_fence();
#pragma unroll
      for (int hh = 0; hh < 2; ++hh) {
        f32x4 s0 = Z4, s1 = Z4, s2 = Z4, s3 = Z4;
#pragma unroll
        for (int u = 0; u < 2; ++u) {
#pragma unroll
          for (int ks = 0; ks < 8; ks += 4) {
            s0 = mfma(cf[8 * u + ks], qf[u][ks], s0); s1 = mfma(cf[8 * u + ks + 1], qf[u][ks + 1], s1);
            s2 = mfma(cf[8 * u + ks + 2], qf[u][ks + 2], s2); s3 = mfma(cf[8 * u + ks + 3], qf[u][ks + 3], s3);
          }
          if (hh == 0) {
#pragma unroll
            for (int ks = 0; ks < 8; ++ks) qf[u][ks] = *(const LAS u32x4*)(size_t)(qad[ks & 3] + (2 * u + (ks >> 2)) * 256 + 16384);
          }
          sched_fence();
        }
        sa[hh] = (s0 + s1) + (s2 + s3);
      }
    }
    float sc[2][4], mx[2];
#pragma unroll
    for (int hh = 0; hh < 2; ++hh) {
#pragma unroll
      for (int r = 0; r < 4; r += 2) {
        const f32x2 t = rb2(rb2((f32x2){sa[hh][r], sa[hh][r + 1]}) * 0.08838834764831845f);
        sc[hh][r] = t[0]; sc[hh][r + 1] = t[1];
      }
      mx[hh] = x32_max(x16_max(mx2(mx3(sc[hh][0], sc[hh][1], sc[hh][2]), sc[hh][3])));
    }
    if (g < 2) *(LAS float*)(sm + L_MX + (16 * g + n) * 32 + w * 4) = g ? mx[1] : mx[0];
    TS(9);
    bar();
    TS(15);
#pragma unroll
    for (int hh = 0; hh < 2; ++hh) {
      f32x4 m0 = *(const LAS f32x4*)(sm + L_MX + (16 * hh + n) * 32), m1 = *(const LAS f32x4*)(sm + L_MX + (16 * hh + n) * 32 + 16);
      mx[hh] = mx3(mx3(m0[0], m0[1], m0[2]), mx3(m0[3], m1[0], m1[1]), mx2(m1[2], m1[3]));
    }
    float ls[2];
#pragma unroll
    for (int hh = 0; hh < 2; ++hh) {
      float p[4];
#pragma unroll
      for (int r = 0; r < 4; ++r) p[r] = __expf(sc[hh][r] - mx[hh]);
      *(LAS u32x2*)(sm + L_P + (16 * hh + n) * 272 + (16 * w + 4 * g) * 2) = (u32x2){pk2(p[0], p[1]), pk2(p[2], p[3])};
      ls[hh] = x32_sum(x16_sum((p[0] + p[1]) + (p[2] + p[3])));
    }
    if (g < 2) *(LAS float*)(sm + L_LS + (16 * g + n) * 32 + w * 4) = g ? ls[1] : ls[0];
    bar();
    TS(7);
#pragma unroll
    for (int hh = 0; hh < 2; ++hh) {
      f32x4 o[4] = {Z4, Z4, Z4, Z4};
#pragma unroll
      for (int ks = 0; ks < 4; ++ks) {
        u32x4 pf = *(const LAS u32x4*)(sm + L_P + (16 * hh + n) * 272 + (32 * ks + 8 * g) * 2);
#pragma unroll
        for (int t = 0; t < 4; ++t) o[t] = mfma(ca[4 * ks + t], pf, o[t]);
      }
      f32x4 l0 = *(const LAS f32x4*)(sm + L_LS + (16 * hh + n) * 32), l1 = *(const LAS f32x4*)(sm + L_LS + (16 * hh + n) * 32 + 16);
      const u32x4 pa = {pk2(o[0][0], o[0][1]), pk2(o[0][2], o[0][3]), pk2(o[1][0], o[1][1]), pk2(o[1][2], o[1][3])};
      const u32x4 pb = {pk2(o[2][0], o[2][1]), pk2(o[2][2], o[2][3]), pk2(o[3][0], o[3][1]), pk2(o[3][2], o[3][3])};
      u32x4 px;
#pragma unroll
      for (int i = 0; i < 4; ++i) px[i] = dppu<0x128>(pb[i]);
      const u32 r0 = OFF_OP + (u32)(((16 * hh + (n & 7)) * 8 + w) * 128 + chunk) * 128 + g * 32;
      wst16(R, r0 + (n & 8) * 2, (n & 8) ? px : pa);
      wst16(R, r0 + 8 * 8 * 128 * 128 + 16 - (n & 8) * 2, (n & 8) ? pa : px);
      u32 rs = sum4(pa) + sum4(pb);
      { auto t = __builtin_amdgcn_permlane16_swap(rs, rs, false, false); rs = t[0] + t[1]; }
      { auto t = __builtin_amdgcn_permlane32_swap(rs, rs, false, false); rs = t[0] + t[1]; }
      const float lt = ((l0[0] + l0[1]) + (l0[2] + l0[3])) + ((l1[0] + l1[1]) + (l1[2] + l1[3]));
      const u32 um = __float_as_uint(mx[hh]), ul = __float_as_uint(lt);
      if (g == 0) wst16(R, OFF_ML + (u32)((w * 128 + chunk) * 32 + 16 * hh + n) * 16, (u32x4){um, ul, rs + E, um + ul + E});
    }
    bar();
    TS(8);
  }
  const u32 t8 = E & 0xFFu, t16 = E & 0xFFFFu;
  if (cls == 0) {
    const u32 base = (u32)(h2 * 8 + 2 * cq) * 128;
    const u32 s0i = 16 * w + (l >> 3);
    const u32 oo = OFF_OP + (base + s0i) * 128 + (l & 7) * 16;
    const u32 kq = OFF_ML + ((2 * cq * 128 + s0i) * 32 + h2) * 16;
    u32x4 o[4], k[4];
    vm_wait0();
    bar();
    TS(4);
    u32x4 Fv[8], Fo[16];
    tr8x8(Wv, Fv);
    read_tr(Fo);
#pragma unroll
    for (int i = 0; i < 16; ++i) pin(Fo[i]);
#pragma unroll
    for (int i = 0; i < 8; ++i) pin(Fv[i]);
    TS(5);
    if (OP_TH) {
      if (w == 0) {
        const u32 go = OFF_ML + ((2 * cq * 128 + l) * 32 + h2) * 16;
        for (int it = 0; it < SPIN_MAX; ++it) {
          const u32 z = opq(go);
          const u32x4 g0 = wld16(R, z), g1 = wld16(R, z + 64 * 512);
          const int c = __builtin_popcountll(__ballot(g0[0] + g0[1] + E == g0[3])) + __builtin_popcountll(__ballot(g1[0] + g1[1] + E == g1[3]));
          if (c >= OP_TH) break;
          __builtin_amdgcn_s_sleep(OP_PS);
        }
      }
      bar();
    }
    TS(6);
#pragma unroll
    for (int i = 0; i < 4; ++i) o[i] = wld16(R, oo + (i & 1) * 1024 + (i >> 1) * 16384);
#pragma unroll
    for (int i = 0; i < 4; ++i) k[i] = wld16(R, kq + (i & 1) * 4096 + (i >> 1) * 65536);
    for (int it = 0; it < SPIN_MAX; ++it) {
      bool nb[4];
#pragma unroll
      for (int i = 0; i < 4; ++i) nb[i] = (sum8l(sum4(o[i])) + E != k[i][2]) | (k[i][0] + k[i][1] + E != k[i][3]);
      if (!__any(nb[0] | nb[1] | nb[2] | nb[3])) break;
      const u32 z = opq(0u);
#pragma unroll
      for (int i = 0; i < 4; ++i)
        if (nb[i]) { o[i] = wld16(R, oo + z + (i & 1) * 1024 + (i >> 1) * 16384); k[i] = wld16(R, kq + z + (i & 1) * 4096 + (i >> 1) * 65536); }
    }
    TS(9);
    float Mw = mx2(__uint_as_float(k[0][0]), __uint_as_float(k[1][0]));
    Mw = mx2(Mw, dpp<0x128>(Mw));
    Mw = x32_max(x16_max(Mw));
    const float w0 = __expf(__uint_as_float(k[0][0]) - Mw), w1 = __expf(__uint_as_float(k[1][0]) - Mw);
    float Lw = w0 * __uint_as_float(k[0][1]) + w1 * __uint_as_float(k[1][1]);
    Lw += dpp<0x128>(Lw);
    Lw = x32_sum(x16_sum(Lw));
    if (l == 0) *(LAS f32x2*)(sm + L_QB + w * 8) = (f32x2){Mw, Lw};
    f32x2 a[8];
#pragma unroll
    for (int i = 0; i < 4; ++i) {
      a[i] = (f32x2){bflo(o[0][i]), bfhi(o[0][i])} * w0 + (f32x2){bflo(o[1][i]), bfhi(o[1][i])} * w1;
      a[4 + i] = (f32x2){bflo(o[2][i]), bfhi(o[2][i])} * w0 + (f32x2){bflo(o[3][i]), bfhi(o[3][i])} * w1;
    }
    float t8v[8];
#pragma unroll
    for (int i = 0; i < 8; ++i) {
      auto sw = __builtin_amdgcn_permlane16_swap(__float_as_uint(a[i >> 1][i & 1]), __float_as_uint(a[4 + (i >> 1)][i & 1]), false, false);
      t8v[i] = __uint_as_float(sw[0]) + __uint_as_float(sw[1]);
    }
    f32x4 r4;
#pragma unroll
    for (int i = 0; i < 4; ++i) {
      auto sw = __builtin_amdgcn_permlane32_swap(__float_as_uint(t8v[i]), __float_as_uint(t8v[4 + i]), false, false);
      const float t = __uint_as_float(sw[0]) + __uint_as_float(sw[1]);
      r4[i] = t + dpp<0x128>(t);
    }
    if (n < 8) *(LAS f32x4*)(sm + L_RED + w * 512 + (g & 1) * 256 + n * 32 + (g >> 1) * 16) = r4;
    TS(11);
    bar();
    TS(12);
    if (w < 4) {
      const LAS f32x4* r = (const LAS f32x4*)(sm + L_RED + (n & 1) * 2048 + (w >> 1) * 256 + (16 * g + 8 * ((n >> 1) & 1)) * 4);
      const f32x4 e0 = *(const LAS f32x4*)(sm + L_QB + (n & 1) * 32), e1 = *(const LAS f32x4*)(sm + L_QB + (n & 1) * 32 + 16);
      const float M = mx2(mx3(e0[0], e0[2], e1[0]), e1[2]);
      float f0 = __expf(e0[0] - M), f1 = __expf(e0[2] - M), f2 = __expf(e1[0] - M), f3 = __expf(e1[2] - M);
      const float inv = rcp((f0 * e0[1] + f1 * e0[3]) + (f2 * e1[1] + f3 * e1[3]));
      f0 *= inv; f1 *= inv; f2 *= inv; f3 *= inv;
      f32x4 lo = (r[0] * f0 + r[32] * f1) + (r[64] * f2 + r[96] * f3), hi = (r[1] * f0 + r[33] * f1) + (r[65] * f2 + r[97] * f3);
      const u32x4 af = {pk2(lo[0], lo[1]), pk2(lo[2], lo[3]), pk2(hi[0], hi[1]), pk2(hi[2], hi[3])};
      float v0[8], v1[8];
#pragma unroll
      for (int i = 0; i < 8; ++i) {
        f32x4 c = mfma(af, Fv[i], Z4);
        v0[i] = c[0] + dpp<0x128>(c[2]);
        v1[i] = c[1] + dpp<0x128>(c[3]);
      }
      if (l < 8) {
        LAS char* d = sm + L_HB + (w >> 1) * 512 + (w & 1) * 128 + l * 16;
        *(LAS u32x4*)d = (u32x4){pk2(v0[0], v0[1]), pk2(v0[2], v0[3]), pk2(v0[4], v0[5]), pk2(v0[6], v0[7])};
        *(LAS u32x4*)(d + 256) = (u32x4){pk2(v1[0], v1[1]), pk2(v1[2], v1[3]), pk2(v1[4], v1[5]), pk2(v1[6], v1[7])};
      }
    }
    bar();
    TS(13);
    {
      f32x4 acc[4] = {Z4, Z4, Z4, Z4};
#pragma unroll
      for (int ks = 0; ks < 4; ++ks) {
        u32x4 vf = *(const LAS u32x4*)(sm + L_HB + (n & 3) * 256 + ks * 64 + g * 16);
#pragma unroll
        for (int i = 0; i < 4; ++i) acc[i] = mfma(Fo[4 * ks + i], vf, acc[i]);
      }
      u32x4 ov;
#pragma unroll
      for (int r = 0; r < 4; ++r) {
        const float lo = (n & 4) ? acc[1][r] : acc[0][r], hi = (n & 4) ? acc[3][r] : acc[2][r];
        const float v = (n & 8) ? hi : lo;
        ov[r] = tg8(v + dpp<0x4E>(v), t8);
      }
      if (!(n & 2)) wst16(R, OFF_OUTP + (u32)((n & 1) * 16 + 2 * w + (n >> 3)) * 16384 + chunk * 128 + ((n >> 2) & 1) * 64 + g * 16, ov);
    }
    TS(10);
  } else if (chunk < 32) {
    const int b = chunk >> 4, m = (chunk & 15) * 32 + (l & 31);
    const u32 oo = OFF_OUTP + (u32)chunk * 16384 + w * 2048 + l * 16;
    const u32 go = OFF_MODG + (u32)xcd * 16384 + b * 2048 + m * 4;
    u32 ga = wld4(R, go), gb = wld4(R, go + 12288);
    const float xx = bflo(gld2(x + b * 512 + m));
    for (int it = 0; it < SPIN_MAX; ++it) {
      if (!__any((((ga ^ E) | (gb ^ E)) & 0xFFFFu) != 0u)) break;
      const u32 o = opq(go);
      ga = wld4(R, o); gb = wld4(R, o + 12288);
    }
    u32x4 v0 = wld16(R, oo), v1 = wld16(R, oo + 1024);
    for (int it = 0; it < SPIN_MAX; ++it) {
      const bool n0 = bad8(v0, E), n1 = bad8(v1, E);
      if (!__any(n0 | n1)) break;
      __builtin_amdgcn_s_sleep(1);
      const u32 o = opq(oo);
      if (n0) v0 = wld16(R, o);
      if (n1) v1 = wld16(R, o + 1024);
    }
    TS(11);
    f32x4 s4;
#pragma unroll
    for (int i = 0; i < 4; ++i) { float t = un8(v0[i]) + un8(v1[i]); t += dpp<0x128>(t); s4[i] = x32_sum(x16_sum(t)); }
    if (l < 8) *(LAS f32x4*)(sm + L_RED + w * 128 + l * 16) = s4;
    bar();
    const LAS float* r = (const LAS float*)(sm + L_RED + (l & 31) * 4);
    const float at = rb(((r[0] + r[32]) + (r[64] + r[96])) + ((r[128] + r[160]) + (r[192] + r[224])));
    const float x1 = rb(xx + rb(bfhi(ga) * at));
    if (l < 32) wst4(R, OFF_X1 + (u32)w * 4096 + b * 2048 + m * 4, tg16(x1, t16));
    if (chunk == 0 && tid == 0) wst4(R, 0, E);
    TS(12);
    const u32 fo = OFF_FIN + (u32)chunk * 8192 + w * 896 + (l < 56 ? l : l - 56) * 16;
    u32x4 f = wld16(R, fo);
    for (int it = 0; it < SPIN_MAX; ++it) {
      if (!__any(bad8(f, E))) break;
      __builtin_amdgcn_s_sleep(1);
      f = wld16(R, opq(fo));
    }
    TS(13);
#pragma unroll
    for (int i = 0; i < 4; ++i) { float t = l < 56 ? un8(f[i]) : 0.f; t += dpp<0x128>(t); s4[i] = x32_sum(x16_sum(t)); }
    if (l < 8) *(LAS f32x4*)(sm + L_RED + 1024 + w * 128 + l * 16) = s4;
    bar();
    if (w == 0) {
      const float y = rb(((r[256] + r[288]) + (r[320] + r[352])) + ((r[384] + r[416]) + (r[448] + r[480])));
      const float o = x1 + rb(bfhi(gb) * y);
      if (l < 32) *(GAS u16*)(out + b * 512 + m) = (u16)(pk2(o, o) & 0xFFFFu);
    }
    TS(14);
  } else if (chunk < 64) {
    const u32 pm = mod_cols(chunk - 16);
    const u32 o = OFF_MODG + (u32)w * 16384 + ((chunk - 32) >> 3) * 4096 + ((chunk & 7) * 64 + l) * 4;
    wst4(R, o, (pm << 16) | t16); wst4(R, o + 2048, (pm & 0xFFFF0000u) | t16);
    TS(10);
  } else if (fi >= 0) {
    u32x4 W[8], FG[8];
    {
      const u16* p = (colblk < 4 ? wg : wu) + (size_t)(64 * w + 32 * kg + 8 * g) * 1792 + fi * 32 + 8 * (colblk & 3);
#pragma unroll
      for (int i = 0; i < 8; ++i) W[i] = gld16(p + i * 1792);
    }
    u32x2 Wd[8];
    {
      const u16* p = wd + (size_t)(fi * 32 + 8 * g) * 512 + 64 * w + 4 * n;
#pragma unroll
      for (int i = 0; i < 8; ++i) Wd[i] = gl8<NT_W1>(p + i * 512);
    }
    sched_fence();
    u32x4 Fd[4];
    tr8x8(W, FG);
    tr8x4(Wd, Fd);
#pragma unroll
    for (int i = 0; i < 8; ++i) pin(FG[i]);
#pragma unroll
    for (int i = 0; i < 4; ++i) pin(Fd[i]);
    TS(10);
    if (w < 2) {
      const u32 so = OFF_MODG + (u32)xcd * 16384 + 4096 + (u32)w * 2048 + l * 16;
      u32x4 sb0 = wld16(R, so), sb1 = wld16(R, so + 1024), cb0 = wld16(R, so + 4096), cb1 = wld16(R, so + 5120);
      for (int it = 0; it < SPIN_MAX; ++it) {
        if (!__any(bad16(sb0, E) | bad16(sb1, E) | bad16(cb0, E) | bad16(cb1, E))) break;
        const u32 o = opq(so);
        sb0 = wld16(R, o); sb1 = wld16(R, o + 1024); cb0 = wld16(R, o + 4096); cb1 = wld16(R, o + 5120);
      }
      float c1[8], sh[8];
#pragma unroll
      for (int i = 0; i < 4; ++i) {
        c1[i] = rb(1.f + bfhi(cb0[i])); c1[4 + i] = rb(1.f + bfhi(cb1[i]));
        sh[i] = bfhi(sb0[i]); sh[4 + i] = bfhi(sb1[i]);
      }
#pragma unroll
      for (int i = 0; i < 8; ++i) { pin(c1[i]); pin(sh[i]); }
      TS(11);
      const u32 xo = OFF_X1 + (u32)xcd * 4096 + (u32)w * 2048 + l * 16;
      u32x4 a0 = wld16(R, xo), a1 = wld16(R, xo + 1024);
      for (int it = 0; it < SPIN_MAX; ++it) {
        if (!__any(bad16(a0, E) | bad16(a1, E))) break;
        const u32 o = opq(xo);
        a0 = wld16(R, o); a1 = wld16(R, o + 1024);
      }
      TS(12);
      f32x2 xv[4];
#pragma unroll
      for (int i = 0; i < 2; ++i) { xv[i] = (f32x2){bfhi(a0[2 * i]), bfhi(a0[2 * i + 1])}; xv[2 + i] = (f32x2){bfhi(a1[2 * i]), bfhi(a1[2 * i + 1])}; }
      const f32x2 s2 = (xv[0] + xv[1]) + (xv[2] + xv[3]);
      const float m = wave_sum(s2[0] + s2[1]) * (1.f / 512.f);
      f32x2 q2 = {0.f, 0.f};
#pragma unroll
      for (int i = 0; i < 4; ++i) { const f32x2 d = xv[i] - m; q2 += d * d; }
      const float var = wave_sum(q2[0] + q2[1]) * (1.f / 512.f);
      const float mean = rb(m);
      f32x2 dv[4];
#pragma unroll
      for (int i = 0; i < 4; ++i) dv[i] = rb2(xv[i] - mean);
      const float sd = rcp(rb(__builtin_amdgcn_sqrtf(rb(rb(var) + 1e-5f))));
      u32 hp[4];
#pragma unroll
      for (int i = 0; i < 4; ++i) {
        const f32x2 t = rb2(rb2(dv[i] * sd) * (f32x2){c1[2 * i], c1[2 * i + 1]}) + (f32x2){sh[2 * i], sh[2 * i + 1]};
        hp[i] = pk2(t[0], t[1]);
      }
      *(LAS u32x2*)(sm + L_HB + w * 1024 + l * 8) = (u32x2){hp[0], hp[1]};
      *(LAS u32x2*)(sm + L_HB + w * 1024 + 512 + l * 8) = (u32x2){hp[2], hp[3]};
    }
    bar();
    {
      u32x4 af = *(const LAS u32x4*)(sm + L_HB + (n & 1) * 1024 + (64 * w + 32 * ((n >> 1) & 1) + 8 * g) * 2);
      float v0[8], v1[8];
#pragma unroll
      for (int i = 0; i < 8; ++i) {
        f32x4 a = mfma(af, FG[i], Z4);
        v0[i] = a[0] + dpp<0x128>(a[2]);
        v1[i] = a[1] + dpp<0x128>(a[3]);
      }
      if (l < 8) {
        LAS f32x4* r = (LAS f32x4*)(sm + L_RED + 4096 + w * 512 + l * 32);
        r[0] = (f32x4){v0[0], v0[1], v0[2], v0[3]}; r[1] = (f32x4){v0[4], v0[5], v0[6], v0[7]};
        r[16] = (f32x4){v1[0], v1[1], v1[2], v1[3]}; r[17] = (f32x4){v1[4], v1[5], v1[6], v1[7]};
      }
    }
    bar();
    {
      const LAS float* r = (const LAS float*)(sm + L_RED + 4096 + (l >> 5) * 256 + (l & 31) * 4);
      float gs = ((r[0] + r[128]) + (r[256] + r[384])) + ((r[512] + r[640]) + (r[768] + r[896]));
      float us = ((r[32] + r[160]) + (r[288] + r[416])) + ((r[544] + r[672]) + (r[800] + r[928]));
      float hv = rb(gelu_f(rb(gs))) * rb(us);
      *(LAS u16*)(sm + L_P + w * 128 + l * 2) = (u16)(pk2(hv, hv) & 0xFFFFu);
      lds_wait();
      u32x4 ha = *(const LAS u32x4*)(sm + L_P + w * 128 + (n & 1) * 64 + g * 16);
      u32x4 o;
#pragma unroll
      for (int i = 0; i < 4; ++i) {
        f32x4 a = mfma(ha, Fd[i], Z4);
        o[i] = tg8((g & 1) ? a[1] : a[0], t8);
      }
      if (g < 2) wst16(R, OFF_FIN + (u32)(g * 16 + 2 * w + (n >> 3)) * 8192 + fi * 128 + (n & 7) * 16, o);
    }
    TS(13);
  }
  if (PF_CODE) asm volatile(".Lwc_b:");
}

extern "C" void kernel_launch(void* const* d_in, const int* in_sizes, int n_in, void* d_out, int out_size, void* d_ws,
                              size_t ws_size, hipStream_t stream) {
  (void)in_sizes; (void)n_in; (void)out_size; (void)ws_size;
  (void)hipFuncSetAttribute((const void*)fused_kernel, hipFuncAttributeMaxDynamicSharedMemorySize, (int)LDS_BYTES);
  hipLaunchKernelGGL(fused_kernel, dim3(256), dim3(512), LDS_BYTES, stream, (const u16*)d_in[0], (const u16*)d_in[1],
                     (const u16*)d_in[2], (const u16*)d_in[3], (const u16*)d_in[4], (const u16*)d_in[5], (const u16*)d_in[7],
                     (const u16*)d_in[8], (const u16*)d_in[9], (const u16*)d_in[10], (const u16*)d_in[11], (u16*)d_out, (char*)d_ws);
}
```
